```python
import torch

def _softmax(x, axis=-1):
    return torch.softmax(x, dim=axis)

def _gelu(x):
    return torch.nn.functional.gelu(x, approximate='tanh')

def _silu(x):
    return torch.nn.functional.silu(x)

def _sigmoid(x):
    return torch.sigmoid(x)

def _layernorm(x, axis=-1, eps=1e-05):
    m = x.mean(dim=axis, keepdim=True)
    v = x.var(dim=axis, keepdim=True, unbiased=False)
    return (x - m) / torch.sqrt(v + eps)

def _scatter_add(tgt, idx, src, axis=0):
    return tgt.clone().index_add_(axis, idx, src)

def _segment_sum(src, seg, n):
    out = torch.zeros((n,) + src.shape[1:], dtype=src.dtype)
    return out.index_add_(0, seg, src)

def _pad(x, pw):
    flat = tuple((v for pair in reversed(pw) for v in pair))
    return torch.nn.functional.pad(x, flat)
kernel_name = 'parallel_sum__memory_aug_recurrent_mem_tr__dense_activation_gelu__prompt'

def setup_inputs():

    _w = lambda a: (lambda t: t.to(torch.bfloat16) if t.is_floating_point() else t)(torch.as_tensor(a))

    def _A_setup():
        B, S, Dm = (1, 2048, 448)
        H, Dh = (16, 256)
        M, seg = (8, 32)
        seg = min(seg, S)
        S = S // seg * seg
        return {'x': _w((lambda *s: torch.randn(*s, dtype=torch.bfloat16) * 0.05)(B, S, Dm)), 'mem0': _w((lambda *s: torch.randn(*s, dtype=torch.bfloat16) * 0.2)(B, M, Dm)), 'Wq': _w((lambda *s: torch.randn(*s, dtype=torch.bfloat16) * 1.6)(Dm, H, Dh) * Dm ** (-0.5)), 'Wk': _w((lambda *s: torch.randn(*s, dtype=torch.bfloat16) * 1.6)(Dm, H, Dh) * Dm ** (-0.5)), 'Wv': _w((lambda *s: torch.randn(*s, dtype=torch.bfloat16) * 0.2)(Dm, H, Dh) * Dm ** (-0.5)), 'Wo': _w((lambda *s: torch.randn(*s, dtype=torch.bfloat16) * 0.2)(H, Dh, Dm) * (H * Dh) ** (-0.5)), 'seg_len': seg}
    _A_in = _A_setup()

    def _B_setup():
        B, S, D, F = (1, 2048, 448, 1792)
        return {'x': _w((lambda *s: torch.randn(*s, dtype=torch.bfloat16) * 0.05)(B, S, D)), 'w1': _w((lambda *s: torch.randn(*s, dtype=torch.bfloat16) * 0.4)(D, F) * D ** (-0.5)), 'b1': _w((lambda *s: torch.randn(*s, dtype=torch.bfloat16) * 12.8)(F) * 0.01), 'w2': _w((lambda *s: torch.randn(*s, dtype=torch.bfloat16) * 0.1)(F, D) * F ** (-0.5)), 'b2': _w((lambda *s: torch.randn(*s, dtype=torch.bfloat16) * 1.6)(D) * 0.01)}
    _B_in = _B_setup()
    return {'A_x': _A_in['x'], 'A_mem0': _A_in['mem0'], 'A_Wq': _A_in['Wq'], 'A_Wk': _A_in['Wk'], 'A_Wv': _A_in['Wv'], 'A_Wo': _A_in['Wo'], 'A_seg_len': _A_in['seg_len'], 'B_x': _B_in['x'], 'B_w1': _B_in['w1'], 'B_b1': _B_in['b1'], 'B_w2': _B_in['w2'], 'B_b2': _B_in['b2']}

def reference(A_x, A_mem0, A_Wq, A_Wk, A_Wv, A_Wo, A_seg_len, B_x, B_w1, B_b1, B_w2, B_b2):

    def _A_ref(x, mem0, Wq, Wk, Wv, Wo, seg_len):
        B, S, Dm = x.shape
        H, Dh = (Wq.shape[1], Wq.shape[2])
        M = mem0.shape[1]
        scale = Dh ** (-0.5)
        n_seg = S // seg_len
        L = M + seg_len
        idx = torch.arange(L)
        mask = (idx[:, None] < M) | (idx[:, None] >= idx[None, :])
        mem = mem0
        outs = []
        for s in range(n_seg):
            xs = x[:, s * seg_len:(s + 1) * seg_len, :].clone()
            h = torch.concatenate([mem, xs], axis=1)
            q = torch.einsum('btd,dhe->bhte', h, Wq)
            k = torch.einsum('btd,dhe->bhte', h, Wk)
            v = torch.einsum('btd,dhe->bhte', h, Wv)
            scores = torch.einsum('bhqe,bhke->bhqk', q, k) * scale
            scores = torch.where(mask[None, None], scores, -1000000000.0)
            p = _softmax(scores, axis=-1)
            o = torch.einsum('bhqk,bhke->bhqe', p, v)
            o = torch.einsum('bhte,hed->btd', o, Wo)
            mem = o[:, :M, :].clone()
            outs.append(o[:, M:, :])
        return torch.concatenate(outs, axis=1)

    def _B_ref(x, w1, b1, w2, b2):
        h = torch.einsum('bsd,df->bsf', x, w1) + b1
        h = _gelu(h)
        return torch.einsum('bsf,fd->bsd', h, w2) + b2
    x = A_x
    xn = _layernorm(x, axis=-1)
    return x + _A_ref(xn, A_mem0, A_Wq, A_Wk, A_Wv, A_Wo, A_seg_len) + _B_ref(xn, B_w1, B_b1, B_w2, B_b2)
```

```cpp
#include <hip/hip_runtime.h>
#include <hip/hip_bf16.h>
#include <stdint.h>

typedef __attribute__((ext_vector_type(8))) short bf16x8;
typedef __attribute__((ext_vector_type(4))) short bf16x4;
typedef __attribute__((ext_vector_type(4))) float f32x4;
typedef __attribute__((ext_vector_type(2))) float f32x2;
typedef __attribute__((ext_vector_type(4))) unsigned u32x4;
typedef __attribute__((ext_vector_type(2))) unsigned u32x2;
typedef __attribute__((ext_vector_type(2))) __bf16 bf2_t;
typedef unsigned short u16;
typedef __attribute__((address_space(1))) unsigned gu32;

constexpr int S_ = 2048, D_ = 448, H_ = 16, E_ = 256, M_ = 8, NSEG = 64, F_ = 1792;
constexpr int HD = H_ * D_;
constexpr int NW = 2 * HD;
constexpr int AUX_SC1 = 16;
constexpr unsigned SENT = 0xFFFFFFFFu;

constexpr size_t al(size_t x) { return (x + 255) & ~(size_t)255; }
constexpr size_t OFF_MEMB = 0;
constexpr size_t SZ_MEMB = (size_t)(NSEG + 1) * M_ * D_ * 2;
constexpr int SREC = 16384;
constexpr size_t OFF_SB = al(OFF_MEMB + SZ_MEMB);
constexpr size_t SZ_SB = (size_t)NSEG * SREC;
constexpr size_t SZ_HAND = al(OFF_SB + SZ_SB);
constexpr size_t OFF_CNT = SZ_HAND;
constexpr int CNT_Q = 0, CNT_TR = 1, CNT_GY = 2, CNT_MLP = 66, CNT_SSS = 130, CNT_REC = 194, CNT_N = 258;
constexpr size_t OFF_XN = al(OFF_CNT + CNT_N * 64);
constexpr size_t OFF_WT = al(OFF_XN + (size_t)S_ * D_ * 2);
constexpr size_t OFF_GT = al(OFF_WT + (size_t)NW * D_ * 2);
constexpr size_t OFF_YT = al(OFF_GT + (size_t)S_ * HD * 2);
constexpr size_t OFF_W1T = al(OFF_YT + (size_t)S_ * HD * 2);
constexpr size_t OFF_W2T = al(OFF_W1T + (size_t)F_ * D_ * 2);
constexpr size_t OFF_MLP = al(OFF_W2T + (size_t)F_ * D_ * 2);
constexpr size_t OFF_GM = al(OFF_MLP + (size_t)S_ * D_ * 2);
constexpr size_t OFF_ZM = al(OFF_GM + (size_t)NSEG * H_ * M_ * D_ * 2);
constexpr size_t OFF_SSS = al(OFF_ZM + (size_t)NSEG * H_ * M_ * D_ * 2);
constexpr size_t WS_END = al(OFF_SSS + (size_t)NSEG * H_ * 32 * 32 * 4);

__device__ __forceinline__ float bf2f(u16 v) { return __uint_as_float(((unsigned)v) << 16); }
__device__ __forceinline__ u16 f2bf(float f) { __bf16 b = (__bf16)f; return __builtin_bit_cast(u16, b); }
__device__ __forceinline__ float rbf(float f) { return bf2f(f2bf(f)); }
__device__ __forceinline__ unsigned pk2(float a, float b) { f32x2 v = {a, b}; bf2_t r = __builtin_convertvector(v, bf2_t); return __builtin_bit_cast(unsigned, r); }
__device__ __forceinline__ float lo16(unsigned u) { return __uint_as_float(u << 16); }
__device__ __forceinline__ float hi16(unsigned u) { return __uint_as_float(u & 0xFFFF0000u); }
__device__ __forceinline__ float ex2(float x) { return __builtin_amdgcn_exp2f(x); }
__device__ __forceinline__ f32x4 mfma32(bf16x8 a, bf16x8 b, f32x4 c) { return __builtin_amdgcn_mfma_f32_16x16x32_bf16(a, b, c, 0, 0, 0); }
__device__ __forceinline__ f32x4 mfma16(bf16x4 a, bf16x4 b, f32x4 c) { return __builtin_amdgcn_mfma_f32_16x16x16bf16_1k(a, b, c, 0, 0, 0); }
__device__ __forceinline__ bf16x8 ldg8(const u16* p) { return *reinterpret_cast<const bf16x8*>(p); }
__device__ __forceinline__ u32x2 cvt4u(f32x4 v) { u32x2 r; r[0] = pk2(v[0], v[1]); r[1] = pk2(v[2], v[3]); return r; }
__device__ __forceinline__ bf16x4 cvt4(f32x4 v) { return __builtin_bit_cast(bf16x4, cvt4u(v)); }
__device__ __forceinline__ float xsum16(float x) { unsigned u = __float_as_uint(x); u32x2 r = __builtin_amdgcn_permlane16_swap(u, u, false, false); return __uint_as_float(r[0]) + __uint_as_float(r[1]); }
__device__ __forceinline__ float xsum32(float x) { unsigned u = __float_as_uint(x); u32x2 r = __builtin_amdgcn_permlane32_swap(u, u, false, false); return __uint_as_float(r[0]) + __uint_as_float(r[1]); }
__device__ __forceinline__ float xmax16(float x) { unsigned u = __float_as_uint(x); u32x2 r = __builtin_amdgcn_permlane16_swap(u, u, false, false); return fmaxf(__uint_as_float(r[0]), __uint_as_float(r[1])); }
__device__ __forceinline__ float xmax32(float x) { unsigned u = __float_as_uint(x); u32x2 r = __builtin_amdgcn_permlane32_swap(u, u, false, false); return fmaxf(__uint_as_float(r[0]), __uint_as_float(r[1])); }
__device__ __forceinline__ bool has_sent(u32x4 v) { return v[0] == SENT || v[1] == SENT || v[2] == SENT || v[3] == SENT; }
__device__ __forceinline__ unsigned canon(unsigned v) { return v == SENT ? 0x7FC07FC0u : v; }

__device__ __forceinline__ int otid() { int t = threadIdx.x; asm volatile("" : "+v"(t)); return t; }
struct Params {
  const u16 *x, *mem0, *Wq, *Wk, *Wv, *Wo, *w1, *b1, *w2, *b2;
  u16* out;
  char* ws;
};
typedef __amdgpu_buffer_rsrc_t rsrc_t;
__device__ __forceinline__ rsrc_t ws_rsrc(const Params& p) { return __builtin_amdgcn_make_buffer_rsrc(p.ws, 0, (int)WS_END, 0x00020000); }
__device__ __forceinline__ void st64(rsrc_t rs, unsigned off, u32x2 v) { __builtin_amdgcn_raw_buffer_store_b64(v, rs, (int)off, 0, AUX_SC1); }
__device__ __forceinline__ void st128(rsrc_t rs, unsigned off, u32x4 v) { __builtin_amdgcn_raw_buffer_store_b128(v, rs, (int)off, 0, AUX_SC1); }
__device__ __forceinline__ void st32(rsrc_t rs, unsigned off, unsigned v) { __builtin_amdgcn_raw_buffer_store_b32(v, rs, (int)off, 0, AUX_SC1); }
__device__ __forceinline__ u32x4 ld128(rsrc_t rs, unsigned off) { return __builtin_amdgcn_raw_buffer_load_b128(rs, (int)off, 0, AUX_SC1); }
__device__ __forceinline__ unsigned ld32(rsrc_t rs, unsigned off) { return __builtin_amdgcn_raw_buffer_load_b32(rs, (int)off, 0, AUX_SC1); }
__device__ __forceinline__ bf16x8 ld8s(rsrc_t rs, unsigned off) { return __builtin_bit_cast(bf16x8, ld128(rs, off)); }
__device__ __forceinline__ gu32* cnt_ptr(const Params& p, int i) { return (gu32*)(p.ws + OFF_CNT + (size_t)i * 64); }
constexpr unsigned SPIN_CAP = 1u << 22;

__device__ __forceinline__ void signal_add(const Params& p, int cnt, unsigned v) {
  asm volatile("s_waitcnt vmcnt(0)" ::: "memory");
  __syncthreads();
  if (threadIdx.x == 0) __hip_atomic_fetch_add(cnt_ptr(p, cnt), v, __ATOMIC_RELAXED, __HIP_MEMORY_SCOPE_AGENT);
}
__device__ __forceinline__ void wait_cnt(const Params& p, int cnt, unsigned want) {
  if (threadIdx.x == 0) {
    unsigned spins = 0;
    while (__hip_atomic_load(cnt_ptr(p, cnt), __ATOMIC_RELAXED, __HIP_MEMORY_SCOPE_AGENT) < want) {
      __builtin_amdgcn_s_sleep(64);
      if (++spins > SPIN_CAP) break;
    }
  }
}
__device__ __forceinline__ void acquire_all() {
  if (threadIdx.x == 0) { __builtin_amdgcn_fence(__ATOMIC_ACQUIRE, "agent"); asm volatile("s_waitcnt vmcnt(0)" ::: "memory"); }
  __syncthreads();
}

constexpr int PLD = 136;
__global__ __launch_bounds__(448) void prep_kernel(Params p) {
  __shared__ __attribute__((aligned(16))) u16 sx[112 * PLD];
  __shared__ __attribute__((aligned(16))) u16 sy[224 * PLD];
  const int tid = threadIdx.x, lane = tid & 63, w = tid >> 6, fr = lane & 15, fq = lane >> 4;
  const int b = blockIdx.x;
  const int mat = (b & 7) * 4 + (b >> 6), tr = (b >> 4) & 3, tc = (b >> 3) & 1;
  const int h = mat & 15; const bool isU = mat >= 16;
  const u16* Yop = isU ? p.Wv : p.Wk;
  bf16x8 vx[2][4], vy[2][8];
#pragma unroll
  for (int kh = 0; kh < 2; ++kh) {
#pragma unroll
    for (int u = 0; u < 4; ++u) {
      const int i = tid + u * 448;
      if (isU) vx[kh][u] = ldg8(p.Wo + ((size_t)h * E_ + kh * 128 + i / 14) * D_ + tr * 112 + (i % 14) * 8);
      else vx[kh][u] = ldg8(p.Wq + ((size_t)(tr * 112 + (i >> 4)) * H_ + h) * E_ + kh * 128 + (i & 15) * 8);
    }
#pragma unroll
    for (int u = 0; u < 8; ++u) {
      const int i = tid + u * 448;
      vy[kh][u] = ldg8(Yop + ((size_t)(tc * 224 + (i >> 4)) * H_ + h) * E_ + kh * 128 + (i & 15) * 8);
    }
  }
  {
    constexpr unsigned N16 = (unsigned)((SZ_HAND - M_ * D_ * 2) / 16);
    u32x4 f = {SENT, SENT, SENT, SENT};
    u32x4* dst = (u32x4*)(p.ws + M_ * D_ * 2);
    for (unsigned i = b * 448 + tid; i < N16; i += 256 * 448) dst[i] = f;
  }
  if (b == 255) {
    u16* memb = (u16*)(p.ws + OFF_MEMB);
    for (int i = tid; i < M_ * D_; i += 448) { int r = i / D_, d = i % D_; memb[((d >> 3) * 8 + r) * 8 + (d & 7)] = p.mem0[i]; }
    for (int i = tid; i < CNT_N; i += 448) *(unsigned*)(p.ws + OFF_CNT + (size_t)i * 64) = 0u;
  }
  for (int rr = w; rr < 8; rr += 7) {
    const int row = b * 8 + rr;
    float v[8]; float s = 0.f;
    if (lane < 56) {
      bf16x8 xv = ldg8(p.x + (size_t)row * D_ + lane * 8);
#pragma unroll
      for (int i = 0; i < 8; ++i) { v[i] = bf2f((u16)xv[i]); s += v[i]; }
    } else {
#pragma unroll
      for (int i = 0; i < 8; ++i) v[i] = 0.f;
    }
#pragma unroll
    for (int o = 32; o > 0; o >>= 1) s += __shfl_xor(s, o);
    const float mean = s * (1.f / D_);
    float q = 0.f;
    if (lane < 56) {
#pragma unroll
      for (int i = 0; i < 8; ++i) { float d = v[i] - mean; q += d * d; }
    }
#pragma unroll
    for (int o = 32; o > 0; o >>= 1) q += __shfl_xor(q, o);
    const float var = rbf(q * (1.f / D_));
    const float mb = rbf(mean);
    const float sd = rbf(sqrtf(rbf(var + 1e-5f)));
    if (lane < 56) {
      u32x4 o;
#pragma unroll
      for (int i = 0; i < 4; ++i) o[i] = pk2(rbf(v[2 * i] - mb) / sd, rbf(v[2 * i + 1] - mb) / sd);
      *reinterpret_cast<u32x4*>((u16*)(p.ws + OFF_XN) + (size_t)row * D_ + lane * 8) = o;
    }
  }
  f32x4 acc[7][2] = {};
#pragma unroll
  for (int kh = 0; kh < 2; ++kh) {
    if (kh) __syncthreads();
#pragma unroll
    for (int u = 0; u < 4; ++u) {
      const int i = tid + u * 448;
      if (isU) {
        const int e = i / 14, c = i % 14;
#pragma unroll
        for (int j = 0; j < 8; ++j) sx[(c * 8 + j) * PLD + ((((e >> 3) ^ c) & 15) << 3) + (e & 7)] = (u16)vx[kh][u][j];
      } else *reinterpret_cast<bf16x8*>(&sx[(i >> 4) * PLD + ((((i & 15) ^ (i >> 7)) & 15) << 3)]) = vx[kh][u];
    }
#pragma unroll
    for (int u = 0; u < 8; ++u) { const int i = tid + u * 448; *reinterpret_cast<bf16x8*>(&sy[(i >> 4) * PLD + (i & 15) * 8]) = vy[kh][u]; }
    __syncthreads();
#pragma unroll
    for (int k = 0; k < 4; ++k) {
      bf16x8 yb[2];
#pragma unroll
      for (int j = 0; j < 2; ++j) yb[j] = *reinterpret_cast<const bf16x8*>(&sy[((2 * w + j) * 16 + fr) * PLD + k * 32 + fq * 8]);
#pragma unroll
      for (int i = 0; i < 7; ++i) {
        bf16x8 xa = *reinterpret_cast<const bf16x8*>(&sx[(i * 16 + fr) * PLD + ((((k * 4 + fq) ^ (i * 2 + (fr >> 3))) & 15) << 3)]);
#pragma unroll
        for (int j = 0; j < 2; ++j) acc[i][j] = mfma32(yb[j], xa, acc[i][j]);
      }
    }
  }
  u16* WT = (u16*)(p.ws + OFF_WT);
  const float sc = isU ? 1.f : 0.0625f * 1.4426950408889634f;
#pragma unroll
  for (int i = 0; i < 7; ++i)
#pragma unroll
    for (int j = 0; j < 2; ++j) {
      const int row = (isU ? HD : 0) + h * D_ + tr * 112 + i * 16 + fr;
      const int col = tc * 224 + (2 * w + j) * 16 + fq * 4;
      *reinterpret_cast<bf16x4*>(WT + (size_t)row * D_ + col) = cvt4(acc[i][j] * sc);
    }
}

constexpr int XS_LD = 456;
template <int NROWS>
__device__ __forceinline__ void stage_x_t(const Params& p, u16* xs, int row0) {
  const u16* xn = (const u16*)(p.ws + OFF_XN) + (size_t)row0 * D_;
  constexpr int N = NROWS * 56, IT = (N + 511) / 512;
  const int t = otid();
  bf16x8 v[IT];
#pragma unroll
  for (int u = 0; u < IT; ++u) { const int i = t + u * 512; if (i < N) v[u] = ldg8(xn + (size_t)i * 8); }
#pragma unroll
  for (int u = 0; u < IT; ++u) { const int i = t + u * 512; if (i < N) { const int r = i / 56, c = i % 56; *reinterpret_cast<bf16x8*>(&xs[r * XS_LD + c * 8]) = v[u]; } }
}

template <bool ISG>
__device__ __forceinline__ void producer_t(const Params& p, rsrc_t rs, char* smem, int nt) {
  const int tid = otid(), lane = tid & 63, w = tid >> 6, fr = lane & 15, fq = lane >> 4;
  u16* xs = (u16*)smem;
  u16* ot = xs + 2 * 32 * XS_LD;
  const u16* WT = (const u16*)(p.ws + OFF_WT);
  const u16* xn = (const u16*)(p.ws + OFF_XN);
  bf16x8 wb[14];
#pragma unroll
  for (int k = 0; k < 14; ++k) wb[k] = ldg8(WT + (size_t)(nt * 128 + w * 16 + fr) * D_ + k * 32 + fq * 8);
  bf16x8 xv[4];
#pragma unroll
  for (int u = 0; u < 4; ++u) { const int i = tid + u * 512; if (i < 1792) xv[u] = ldg8(xn + (size_t)i * 8); }
  for (int s = 0; s < NSEG; ++s) {
    u16* xb = xs + (s & 1) * 32 * XS_LD;
#pragma unroll
    for (int u = 0; u < 4; ++u) { const int i = tid + u * 512; if (i < 1792) { const int r = i / 56, c = i % 56; *reinterpret_cast<bf16x8*>(&xb[r * XS_LD + c * 8]) = xv[u]; } }
    asm volatile("s_waitcnt vmcnt(0)" ::: "memory");
    __syncthreads();
    if (s > 0 && tid == 0) __hip_atomic_fetch_add(cnt_ptr(p, CNT_GY + s - 1), 1u, __ATOMIC_RELAXED, __HIP_MEMORY_SCOPE_AGENT);
    if (s + 1 < NSEG) {
#pragma unroll
      for (int u = 0; u < 4; ++u) { const int i = tid + u * 512; if (i < 1792) xv[u] = ldg8(xn + (size_t)(s + 1) * 32 * D_ + (size_t)i * 8); }
    }
    f32x4 acc[2] = {};
#pragma unroll
    for (int k = 0; k < 14; ++k)
#pragma unroll
      for (int i = 0; i < 2; ++i) {
        bf16x8 xa = *reinterpret_cast<const bf16x8*>(&xb[(i * 16 + fr) * XS_LD + k * 32 + fq * 8]);
        acc[i] = ISG ? mfma32(wb[k], xa, acc[i]) : mfma32(xa, wb[k], acc[i]);
      }
    if (ISG) {
#pragma unroll
      for (int i = 0; i < 2; ++i) *reinterpret_cast<u32x2*>(&ot[(i * 16 + fr) * 136 + w * 16 + fq * 4]) = cvt4u(acc[i]);
    } else {
#pragma unroll
      for (int i = 0; i < 2; ++i) *reinterpret_cast<u32x2*>(&ot[(w * 16 + fr) * 36 + i * 16 + fq * 4]) = cvt4u(acc[i]);
    }
    __syncthreads();
    if (ISG) {
      const int r = tid >> 4, cch = tid & 15;
      const u32x2 a = *reinterpret_cast<const u32x2*>(&ot[r * 136 + cch * 8]), b2 = *reinterpret_cast<const u32x2*>(&ot[r * 136 + cch * 8 + 4]);
      u32x4 v = {a[0], a[1], b2[0], b2[1]};
      st128(rs, (unsigned)OFF_GT + (unsigned)((s * 32 + r) * HD + nt * 128 + cch * 8) * 2u, v);
    } else {
      const int row = tid >> 2, cch = tid & 3;
      const u32x2 a = *reinterpret_cast<const u32x2*>(&ot[row * 36 + cch * 8]), b2 = *reinterpret_cast<const u32x2*>(&ot[row * 36 + cch * 8 + 4]);
      u32x4 v = {a[0], a[1], b2[0], b2[1]};
      st128(rs, (unsigned)OFF_YT + (unsigned)((s * HD + nt * 128 - HD + row) * 32 + cch * 8) * 2u, v);
    }
  }
  signal_add(p, CNT_GY + NSEG - 1, 1u);
}

__device__ __forceinline__ void tr_item(const Params& p, rsrc_t rs, char* smem, int it) {
  const int tid = otid();
  u16* sx = (u16*)smem;
  for (int t = it * 8; t < it * 8 + 8; ++t) {
    const bool second = t >= 196; const int tt = second ? t - 196 : t;
    const u16* src = second ? p.w2 : p.w1;
    const unsigned dst = (unsigned)(second ? OFF_W2T : OFF_W1T);
    const int R = second ? F_ : D_, C = second ? D_ : F_;
    const int nct = C / 64, rt = tt / nct, ct = tt % nct;
    __syncthreads();
    { const int r = tid >> 3, c = tid & 7;
      bf16x8 v = ldg8(src + (size_t)(rt * 64 + r) * C + ct * 64 + c * 8);
#pragma unroll
      for (int j = 0; j < 8; ++j) sx[(c * 8 + j) * 72 + r] = (u16)v[j]; }
    __syncthreads();
    { const int c = tid >> 3, r = tid & 7;
      st128(rs, dst + (unsigned)((ct * 64 + c) * R + rt * 64 + r * 8) * 2u, *reinterpret_cast<const u32x4*>(&sx[c * 72 + r * 8])); }
  }
  signal_add(p, CNT_TR, 1u);
}

constexpr int HS_LD = 1800;
__device__ __forceinline__ float gelu_tanh(float x) {
  const float u = 0.7978845608028654f * (x + 0.044715f * x * x * x);
  const float e = __expf(2.f * u);
  const float th = 1.f - 2.f / (e + 1.f);
  return 0.5f * x * (1.f + th);
}
__device__ __forceinline__ void mlp_item(const Params& p, rsrc_t rs, char* smem, int rb) {
  const int tid = otid(), lane = tid & 63, w = tid >> 6, fr = lane & 15, fq = lane >> 4;
  u16* xs = (u16*)smem;
  u16* hs = xs + 32 * XS_LD;
  wait_cnt(p, CNT_TR, 49u);
  acquire_all();
  stage_x_t<32>(p, xs, rb * 32);
  __syncthreads();
  const u16* w1T = (const u16*)(p.ws + OFF_W1T);
  for (int nt = w; nt < 112; nt += 16) {
    f32x4 acc[2][2] = {};
    bf16x8 wb[2][14];
#pragma unroll
    for (int u = 0; u < 2; ++u)
#pragma unroll
      for (int k = 0; k < 14; ++k) wb[u][k] = ldg8(w1T + (size_t)((nt + 8 * u) * 16 + fr) * D_ + k * 32 + fq * 8);
#pragma unroll
    for (int k = 0; k < 14; ++k)
#pragma unroll
      for (int i = 0; i < 2; ++i) {
        bf16x8 xa = *reinterpret_cast<const bf16x8*>(&xs[(i * 16 + fr) * XS_LD + k * 32 + fq * 8]);
#pragma unroll
        for (int u = 0; u < 2; ++u) acc[u][i] = mfma32(wb[u][k], xa, acc[u][i]);
      }
#pragma unroll
    for (int u = 0; u < 2; ++u) {
      const int n = (nt + 8 * u) * 16 + fq * 4;
      bf16x4 bv = *reinterpret_cast<const bf16x4*>(p.b1 + n);
#pragma unroll
      for (int i = 0; i < 2; ++i) {
        f32x4 g;
#pragma unroll
        for (int r = 0; r < 4; ++r) g[r] = gelu_tanh(rbf(rbf(acc[u][i][r]) + bf2f((u16)bv[r])));
        *reinterpret_cast<bf16x4*>(&hs[(i * 16 + fr) * HS_LD + n]) = cvt4(g);
      }
    }
  }
  __syncthreads();
  const u16* w2T = (const u16*)(p.ws + OFF_W2T);
  const int kh = w & 1, ng = w >> 1;
  f32x4 acc[2][7] = {};
  for (int k4 = 0; k4 < 7; ++k4) {
    bf16x8 wb[4][7];
#pragma unroll
    for (int u = 0; u < 4; ++u)
#pragma unroll
      for (int j = 0; j < 7; ++j) wb[u][j] = ldg8(w2T + (size_t)((ng * 7 + j) * 16 + fr) * F_ + kh * 896 + (k4 * 4 + u) * 32 + fq * 8);
#pragma unroll
    for (int u = 0; u < 4; ++u) {
      const int kk = kh * 896 + (k4 * 4 + u) * 32 + fq * 8;
#pragma unroll
      for (int i = 0; i < 2; ++i) {
        bf16x8 ha = *reinterpret_cast<const bf16x8*>(&hs[(i * 16 + fr) * HS_LD + kk]);
#pragma unroll
        for (int j = 0; j < 7; ++j) acc[i][j] = mfma32(wb[u][j], ha, acc[i][j]);
      }
    }
  }
  __syncthreads();
  float* red = (float*)smem;
  if (kh == 1) {
#pragma unroll
    for (int i = 0; i < 2; ++i)
#pragma unroll
      for (int j = 0; j < 7; ++j) *reinterpret_cast<f32x4*>(&red[(i * 16 + fr) * D_ + (ng * 7 + j) * 16 + fq * 4]) = acc[i][j];
  }
  __syncthreads();
  if (kh == 0) {
#pragma unroll
    for (int i = 0; i < 2; ++i)
#pragma unroll
      for (int j = 0; j < 7; ++j) {
        const int n = (ng * 7 + j) * 16 + fq * 4;
        f32x4 o = acc[i][j] + *reinterpret_cast<const f32x4*>(&red[(i * 16 + fr) * D_ + n]);
        bf16x4 bv = *reinterpret_cast<const bf16x4*>(p.b2 + n);
#pragma unroll
        for (int r = 0; r < 4; ++r) o[r] = rbf(o[r]) + bf2f((u16)bv[r]);
        st64(rs, (unsigned)OFF_MLP + (unsigned)((rb * 32 + i * 16 + fr) * D_ + n) * 2u, cvt4u(o));
      }
  }
  signal_add(p, CNT_MLP + rb, 1u);
}

__device__ __forceinline__ void sss_item(const Params& p, rsrc_t rs, int s) {
  const int tid = otid(), lane = tid & 63, w = tid >> 6, fr = lane & 15, fq = lane >> 4;
  wait_cnt(p, CNT_GY + s, 112u);
  acquire_all();
  const u16* xn = (const u16*)(p.ws + OFF_XN) + (size_t)s * 32 * D_;
  const u16* Gt = (const u16*)(p.ws + OFF_GT) + (size_t)s * 32 * HD;
  for (int hh = 0; hh < 2; ++hh) {
    const int h = w * 2 + hh;
    f32x4 acc[2][2] = {};
#pragma unroll
    for (int k = 0; k < 14; ++k) {
      bf16x8 xa[2], gb[2];
#pragma unroll
      for (int i = 0; i < 2; ++i) {
        xa[i] = ldg8(xn + (size_t)(i * 16 + fr) * D_ + k * 32 + fq * 8);
        gb[i] = ldg8(Gt + (size_t)(i * 16 + fr) * HD + h * D_ + k * 32 + fq * 8);
      }
#pragma unroll
      for (int i = 0; i < 2; ++i)
#pragma unroll
        for (int j = 0; j < 2; ++j) acc[i][j] = mfma32(gb[j], xa[i], acc[i][j]);
    }
#pragma unroll
    for (int i = 0; i < 2; ++i)
#pragma unroll
      for (int j = 0; j < 2; ++j)
        st128(rs, (unsigned)OFF_SSS + (unsigned)(((s * H_ + h) * 32 + i * 16 + fr) * 32 + j * 16 + fq * 4) * 4u, __builtin_bit_cast(u32x4, acc[i][j]));
  }
  signal_add(p, CNT_SSS + s, 1u);
}

constexpr int PS_LD = 648;
__device__ __forceinline__ void tail_item(const Params& p, char* smem, int s, int cq) {
  const int tid = otid(), lane = tid & 63, w = tid >> 6, fr = lane & 15, fq = lane >> 4;
  float* ssm = (float*)smem;
  u16* ps = (u16*)(smem + 16 * 32 * 8 * 4);
  wait_cnt(p, CNT_SSS + s, 1u);
  wait_cnt(p, CNT_MLP + s, 1u);
  wait_cnt(p, CNT_REC + s, 64u);
  acquire_all();
  const u16* xn = (const u16*)(p.ws + OFF_XN) + (size_t)s * 32 * D_;
  const u16* Gm = (const u16*)(p.ws + OFF_GM) + (size_t)s * H_ * M_ * D_;
  const int hrow = tid >> 5, trow = tid & 31;
  f32x4 sv[8];
  {
    const float* sr = (const float*)(p.ws + OFF_SSS) + (((size_t)s * H_ + hrow) * 32 + trow) * 32;
#pragma unroll
    for (int k = 0; k < 8; ++k) sv[k] = *reinterpret_cast<const f32x4*>(sr + k * 4);
  }
  {
    f32x4 acc[2] = {};
#pragma unroll
    for (int kh = 0; kh < 2; ++kh) {
      bf16x8 gb[7], xa[7][2];
#pragma unroll
      for (int k = 0; k < 7; ++k) {
        { const int d = (kh * 7 + k) * 32 + fq * 8, hh2 = w * 2 + (fr >> 3), jq2 = d / 112, dd = d - jq2 * 112;
          gb[k] = ldg8(Gm + (size_t)((((hh2 * 4 + jq2) * 7 + (dd >> 4)) * 8 + (fr & 7)) * 16 + (dd & 15))); }
#pragma unroll
        for (int i = 0; i < 2; ++i) xa[k][i] = ldg8(xn + (size_t)(i * 16 + fr) * D_ + (kh * 7 + k) * 32 + fq * 8);
      }
#pragma unroll
      for (int k = 0; k < 7; ++k)
#pragma unroll
        for (int i = 0; i < 2; ++i) acc[i] = mfma32(gb[k], xa[k][i], acc[i]);
    }
#pragma unroll
    for (int i = 0; i < 2; ++i)
      *reinterpret_cast<f32x4*>(&ssm[((w * 2 + (fq >> 1)) * 32 + i * 16 + fr) * 8 + (fq & 1) * 4]) = acc[i];
  }
  bf16x8 wb[20];
  const int dcol = cq * 112 + (w < 7 ? w : 0) * 16 + fr;
  {
    const u16* Yt = (const u16*)(p.ws + OFF_YT) + (size_t)s * HD * 32;
    const u16* Zm = (const u16*)(p.ws + OFF_ZM) + (size_t)s * H_ * D_ * M_;
#pragma unroll
    for (int k = 0; k < 20; ++k)
      wb[k] = (k < 16) ? ldg8(Yt + ((size_t)k * D_ + dcol) * 32 + fq * 8) : ldg8(Zm + (size_t)((((dcol >> 3) * 8 + (((k - 16) * 4 + fq) >> 1)) * 16 + (((k - 16) * 4 + fq) & 1) * 8 + (dcol & 7)) * 8));
  }
  __syncthreads();
  {
    const int h = hrow, t = trow;
    float a[32], m[8];
#pragma unroll
    for (int k = 0; k < 8; ++k) { a[4 * k] = sv[k][0]; a[4 * k + 1] = sv[k][1]; a[4 * k + 2] = sv[k][2]; a[4 * k + 3] = sv[k][3]; }
#pragma unroll
    for (int j = 0; j < 8; ++j) m[j] = ssm[(h * 32 + t) * 8 + j];
    float mx = m[0];
#pragma unroll
    for (int j = 1; j < 8; ++j) mx = fmaxf(mx, m[j]);
#pragma unroll
    for (int k = 0; k < 32; ++k) if (k <= t) mx = fmaxf(mx, a[k]);
    float sum = 0.f;
#pragma unroll
    for (int j = 0; j < 8; ++j) { m[j] = ex2(m[j] - mx); sum += m[j]; }
#pragma unroll
    for (int k = 0; k < 32; ++k) { a[k] = (k <= t) ? ex2(a[k] - mx) : 0.f; sum += a[k]; }
    const float inv = 1.f / sum;
#pragma unroll
    for (int k = 0; k < 4; ++k) {
      u32x4 v;
#pragma unroll
      for (int r = 0; r < 4; ++r) v[r] = pk2(a[8 * k + 2 * r] * inv, a[8 * k + 2 * r + 1] * inv);
      *reinterpret_cast<u32x4*>(&ps[t * PS_LD + h * 32 + k * 8]) = v;
    }
    u32x4 v;
#pragma unroll
    for (int r = 0; r < 4; ++r) v[r] = pk2(m[2 * r] * inv, m[2 * r + 1] * inv);
    *reinterpret_cast<u32x4*>(&ps[t * PS_LD + 512 + h * 8]) = v;
  }
  __syncthreads();
  if (w < 7) {
    f32x4 acc[2] = {};
#pragma unroll
    for (int k = 0; k < 20; ++k) {
#pragma unroll
      for (int i = 0; i < 2; ++i) {
        bf16x8 pa = *reinterpret_cast<const bf16x8*>(&ps[(i * 16 + fr) * PS_LD + k * 32 + fq * 8]);
        acc[i] = mfma32(wb[k], pa, acc[i]);
      }
    }
    const u16* mo = (const u16*)(p.ws + OFF_MLP);
#pragma unroll
    for (int i = 0; i < 2; ++i) {
      const size_t o = (size_t)(s * 32 + i * 16 + fr) * D_ + cq * 112 + w * 16 + fq * 4;
      bf16x4 xv = *reinterpret_cast<const bf16x4*>(p.x + o);
      bf16x4 bv = *reinterpret_cast<const bf16x4*>(mo + o);
      f32x4 r;
#pragma unroll
      for (int j = 0; j < 4; ++j) r[j] = rbf(bf2f((u16)xv[j]) + rbf(acc[i][j])) + bf2f((u16)bv[j]);
      *reinterpret_cast<bf16x4*>(p.out + o) = cvt4(r);
    }
  }
  __syncthreads();
}

constexpr int IT_TR = 49, IT_MLP = 113, IT_SSS = 177, IT_END = 433;
__device__ __forceinline__ void worker(const Params& p, char* smem) {
  const rsrc_t rs = ws_rsrc(p);
  unsigned* sitem = (unsigned*)(smem + 152 * 1024 - 16);
  if (blockIdx.x < 64 + 112) {
    const int nt = blockIdx.x - 64;
    if (nt < 56) producer_t<true>(p, rs, smem, nt); else producer_t<false>(p, rs, smem, nt);
  }
  for (;;) {
    __syncthreads();
    if (threadIdx.x == 0) *sitem = __hip_atomic_fetch_add(cnt_ptr(p, CNT_Q), 1u, __ATOMIC_RELAXED, __HIP_MEMORY_SCOPE_AGENT);
    __syncthreads();
    const int it = (int)*sitem;
    if (it >= IT_END) break;
    if (it < IT_TR) tr_item(p, rs, smem, it);
    else if (it < IT_MLP) mlp_item(p, rs, smem, it - IT_TR);
    else if (it < IT_SSS) sss_item(p, rs, it - IT_MLP);
    else { const int q = it - IT_SSS; tail_item(p, smem, q >> 2, q & 3); }
  }
}

__device__ __forceinline__ void recurrence(const Params& p, char* smem) {
  u16* ms = (u16*)smem;
  float* red1 = (float*)(smem + 7168);
  float* red2 = red1 + 7 * 64;
  u16* zs = (u16*)(red2 + 16 * 64);
  const int tid = threadIdx.x, lane = tid & 63, w = tid >> 6, fr = lane & 15, fq = lane >> 4;
  const int c = blockIdx.x, h = c >> 2, jq = c & 3;
  const bool isval = c < 56;
  const u16* WT = (const u16*)(p.ws + OFF_WT);
  const rsrc_t rs = ws_rsrc(p);
  const unsigned g_off = (unsigned)OFF_GT + (unsigned)((jq * 8 + (fr & 7)) * HD + h * D_ + fq * 8) * 2u;
  const int zh = 2 * w + (fr >> 3), zd = (isval ? 8 * c : 0) + (fr & 7);
  const unsigned y_off = (unsigned)OFF_YT + (unsigned)((zh * D_ + zd) * 32 + fq * 8) * 2u;

  bf16x8 wa[14], wz[14];
  if (w < 7) {
#pragma unroll
    for (int k = 0; k < 14; ++k) wa[k] = ldg8(WT + (size_t)(h * D_ + jq * 112 + w * 16 + fr) * D_ + k * 32 + fq * 8);
  }
#pragma unroll
  for (int k = 0; k < 14; ++k) wz[k] = ldg8(WT + (size_t)(HD + zh * D_ + zd) * D_ + k * 32 + fq * 8);
  {
    unsigned spins = 0;
    while (__hip_atomic_load(cnt_ptr(p, CNT_GY), __ATOMIC_RELAXED, __HIP_MEMORY_SCOPE_AGENT) < 112u) { __builtin_amdgcn_s_sleep(2); if (++spins > SPIN_CAP) break; }
  }
  if (w == 7) {
#pragma unroll
    for (int k = 0; k < 14; ++k) wa[k] = ld8s(rs, g_off + k * 64);
  }
  bf16x8 yf = ld8s(rs, y_off);
  __builtin_amdgcn_s_waitcnt(0);
  for (int s = 0; s < NSEG; ++s) {
    if (w < 7) {
      const int id = w * 64 + lane, cc = id >> 3, i = id & 7;
      const unsigned off = (unsigned)OFF_MEMB + s * (M_ * D_ * 2) + id * 16;
      u32x4 v; unsigned spins = 0;
      for (;;) {
        asm volatile("" ::: "memory");
        v = ld128(rs, off);
        if (!__any(has_sent(v))) break;
        if (++spins > SPIN_CAP) break;
      }
      *reinterpret_cast<u32x4*>(&ms[((cc >> 3) * 64 + (cc & 3) * 16 + ((cc >> 2) & 1) * 8 + i) * 8]) = v;
    } else {
      asm volatile("s_waitcnt vmcnt(0)" ::: "memory");
    }
    __syncthreads();
    bf16x8 mf[14];
#pragma unroll
    for (int k = 0; k < 7; ++k) mf[2 * k] = *reinterpret_cast<const bf16x8*>(&ms[(k * 64 + lane) * 8]);
    bf16x4 m4;
    { const int d0 = jq * 112 + (w < 7 ? w : 0) * 16 + fq * 4, cc = d0 >> 3;
      m4 = *reinterpret_cast<const bf16x4*>(&ms[((cc >> 3) * 64 + (cc & 3) * 16 + ((cc >> 2) & 1) * 8 + (fr & 7)) * 8 + (d0 & 4)]); }
    __builtin_amdgcn_sched_barrier(0);
#pragma unroll
    for (int k = 0; k < 7; ++k) {
      u32x4 t = __builtin_bit_cast(u32x4, mf[2 * k]), r;
#pragma unroll
      for (int e = 0; e < 4; ++e) r[e] = __builtin_amdgcn_mov_dpp(t[e], 0x128  , 0xF, 0xF, false);
      mf[2 * k + 1] = __builtin_bit_cast(bf16x8, r);
    }
    f32x4 acc = {}, acc1 = {};
#pragma unroll
    for (int k = 0; k < 7; ++k) { acc = mfma32(wa[2 * k], mf[2 * k], acc); acc1 = mfma32(wa[2 * k + 1], mf[2 * k + 1], acc1); }
    acc += acc1;
    u32x2 gbu = cvt4u(acc);
    bf16x4 gb = __builtin_bit_cast(bf16x4, gbu);
    if (w < 7) {
      f32x4 z4 = {};
      f32x4 sp = mfma16(gb, m4, z4);
      if (fr < 8 && fq < 2) *reinterpret_cast<f32x4*>(&red1[w * 64 + fr * 8 + fq * 4]) = sp;
      if (fr < 8) st64(rs, (unsigned)OFF_GM + (unsigned)((((s * 64 + c) * 7 + w) * 8 + fr) * 16 + fq * 4) * 2u, gbu);
    } else {
      if (fr < 8 && fq < 2) {
        u32x2 o; o[0] = canon(gbu[0]); o[1] = canon(gbu[1]);
        st64(rs, (unsigned)OFF_SB + s * SREC + c * 256 + fr * 16 + fq * 8, o);
      }
    }
    __syncthreads();
    if (w == 0 && lane < 32) {
      f32x2 t = *reinterpret_cast<const f32x2*>(&red1[lane * 2]);
#pragma unroll
      for (int k = 1; k < 7; ++k) t += *reinterpret_cast<const f32x2*>(&red1[k * 64 + lane * 2]);
      st32(rs, (unsigned)OFF_SB + s * SREC + c * 256 + 128 + lane * 4, canon(pk2(t[0], t[1])));
    }
    if (s > 0 && tid == 64) __hip_atomic_fetch_add(cnt_ptr(p, CNT_REC + s - 1), 1u, __ATOMIC_RELAXED, __HIP_MEMORY_SCOPE_AGENT);
    const bool need_gy = (s + 1) < NSEG;
    if (isval) {
      f32x4 az = {};
#pragma unroll
      for (int k = 0; k < 14; ++k) az = mfma32(mf[k], wz[k], az);
      u32x2 zbu = cvt4u(az);
      if (fq < 2) {
        st64(rs, (unsigned)OFF_ZM + (unsigned)((((s * 56 + c) * 8 + w) * 16 + fr) * 8 + fq * 4) * 2u, zbu);
        *reinterpret_cast<u32x2*>(&zs[(w * 16 + fr) * 8 + fq * 4]) = zbu;
      }
      u32x4 zf = {0u, 0u, 0u, 0u};
      zf[0] = *reinterpret_cast<const unsigned*>(&zs[(w * 16 + fr) * 8 + fq * 2]);
      const int hh = fr >> 3, i = fr & 7, hd = 2 * w + hh;
      const unsigned o1 = (unsigned)OFF_SB + s * SREC + (hd * 4 + fq) * 256 + i * 16;
      const unsigned o2 = (unsigned)OFF_SB + s * SREC + hd * 1024 + 128 + i * 16 + fq * 4;
      const unsigned o3 = (unsigned)OFF_CNT + (unsigned)(CNT_GY + (need_gy ? (s + 1) : 0)) * 64u;
      u32x4 a0; unsigned b0, b1, b2, b3, gc; unsigned spins = 0;
      for (;;) {
        asm volatile("" ::: "memory");
        a0 = ld128(rs, o1);
        b0 = ld32(rs, o2); b1 = ld32(rs, o2 + 256); b2 = ld32(rs, o2 + 512); b3 = ld32(rs, o2 + 768);
        gc = ld32(rs, o3);
        if (!__any(has_sent(a0) || b0 == SENT || b1 == SENT || b2 == SENT || b3 == SENT || gc < 112u)) break;
        if (++spins > SPIN_CAP) break;
      }
      float sg[8];
#pragma unroll
      for (int r = 0; r < 4; ++r) { sg[2 * r] = lo16(a0[r]); sg[2 * r + 1] = hi16(a0[r]); }
      float m0 = (lo16(b0) + lo16(b1)) + (lo16(b2) + lo16(b3));
      float m1 = (hi16(b0) + hi16(b1)) + (hi16(b2) + hi16(b3));
      float mx = fmaxf(fmaxf(fmaxf(sg[0], sg[1]), fmaxf(sg[2], sg[3])), fmaxf(fmaxf(sg[4], sg[5]), fmaxf(sg[6], sg[7])));
      mx = fmaxf(mx, fmaxf(m0, m1));
      mx = xmax32(xmax16(mx));
      m0 = ex2(m0 - mx); m1 = ex2(m1 - mx);
      float ss = m0 + m1;
#pragma unroll
      for (int r = 0; r < 8; ++r) { sg[r] = ex2(sg[r] - mx); ss += sg[r]; }
      ss = xsum32(xsum16(ss));
      const float inv = __builtin_amdgcn_rcpf(ss);
      u32x4 pa, pm = {0u, 0u, 0u, 0u};
#pragma unroll
      for (int r = 0; r < 4; ++r) pa[r] = pk2(sg[2 * r] * inv, sg[2 * r + 1] * inv);
      pm[0] = pk2(m0 * inv, m1 * inv);
      f32x4 o = {};
      o = mfma32(yf, __builtin_bit_cast(bf16x8, pa), o);
      o = mfma32(__builtin_bit_cast(bf16x8, zf), __builtin_bit_cast(bf16x8, pm), o);
      if ((fr >> 3) == (fq >> 1)) *reinterpret_cast<f32x4*>(&red2[(w * 2 + (fq >> 1)) * 64 + (fr & 7) * 8 + (fq & 1) * 4]) = o;
      __syncthreads();
      if (w == 0) {
        const int hb = (lane >> 5) * 8, l2 = (lane & 31) * 2;
        f32x2 t = *reinterpret_cast<const f32x2*>(&red2[hb * 64 + l2]);
#pragma unroll
        for (int k = 1; k < 8; ++k) t += *reinterpret_cast<const f32x2*>(&red2[(hb + k) * 64 + l2]);
        const float t0 = xsum32(t[0]), t1 = xsum32(t[1]);
        if (lane < 32) st32(rs, (unsigned)OFF_MEMB + (s + 1) * (M_ * D_ * 2) + c * 128 + lane * 4, canon(pk2(t0, t1)));
      }
      if (s + 1 < NSEG) yf = ld8s(rs, y_off + (unsigned)(s + 1) * (HD * 32 * 2));
    } else if (need_gy) {
      unsigned spins = 0;
      while (__hip_atomic_load(cnt_ptr(p, CNT_GY + s + 1), __ATOMIC_RELAXED, __HIP_MEMORY_SCOPE_AGENT) < 112u) { __builtin_amdgcn_s_sleep(2); if (++spins > SPIN_CAP) break; }
    }
    if (w == 7 && s + 1 < NSEG) {
#pragma unroll
      for (int k = 0; k < 14; ++k) wa[k] = ld8s(rs, g_off + (unsigned)(s + 1) * (32 * HD * 2) + k * 64);
    }
  }
  signal_add(p, CNT_REC + NSEG - 1, 1u);
}

constexpr int LDS_MAIN = 152 * 1024;
__global__ __launch_bounds__(512, 2) void main_kernel(Params p) {
  extern __shared__ __attribute__((aligned(16))) char smem[];
  if (blockIdx.x < 64) recurrence(p, smem);
  else worker(p, smem);
}

extern "C" void kernel_launch(void* const* d_in, const int* in_sizes, int n_in, void* d_out, int out_size, void* d_ws,
                              size_t ws_size, hipStream_t stream) {
  Params p;
  p.x = (const u16*)d_in[0]; p.mem0 = (const u16*)d_in[1]; p.Wq = (const u16*)d_in[2]; p.Wk = (const u16*)d_in[3];
  p.Wv = (const u16*)d_in[4]; p.Wo = (const u16*)d_in[5]; p.w1 = (const u16*)d_in[8]; p.b1 = (const u16*)d_in[9];
  p.w2 = (const u16*)d_in[10]; p.b2 = (const u16*)d_in[11];
  p.out = (u16*)d_out; p.ws = (char*)d_ws;
  (void)hipFuncSetAttribute((const void*)main_kernel, hipFuncAttributeMaxDynamicSharedMemorySize, LDS_MAIN);
  prep_kernel<<<256, 448, 0, stream>>>(p);
  main_kernel<<<256, 512, LDS_MAIN, stream>>>(p);
}
```

```cpp
#include <hip/hip_runtime.h>
#include <hip/hip_bf16.h>
#include <stdint.h>

typedef __attribute__((ext_vector_type(8))) short bf16x8;
typedef __attribute__((ext_vector_type(4))) short bf16x4;
typedef __attribute__((ext_vector_type(4))) float f32x4;
typedef __attribute__((ext_vector_type(2))) float f32x2;
typedef __attribute__((ext_vector_type(4))) unsigned u32x4;
typedef __attribute__((ext_vector_type(2))) unsigned u32x2;
typedef __attribute__((ext_vector_type(2))) __bf16 bf2_t;
typedef unsigned short u16;
typedef __attribute__((address_space(1))) unsigned gu32;

constexpr int S_ = 2048, D_ = 448, H_ = 16, E_ = 256, M_ = 8, NSEG = 64, F_ = 1792;
constexpr int HD = H_ * D_;
constexpr int NW = 2 * HD;
constexpr int AUX_SC1 = 16;
constexpr unsigned SENT = 0xFFFFFFFFu;

constexpr size_t al(size_t x) { return (x + 255) & ~(size_t)255; }
constexpr size_t OFF_MEMB = 0;
constexpr size_t SZ_MEMB = (size_t)(NSEG + 1) * M_ * D_ * 2;
constexpr int SREC = 16384;
constexpr size_t OFF_SB = al(OFF_MEMB + SZ_MEMB);
constexpr size_t SZ_SB = (size_t)NSEG * SREC;
constexpr size_t SZ_HAND = al(OFF_SB + SZ_SB);
constexpr size_t OFF_CNT = SZ_HAND;
constexpr int CNT_Q = 0, CNT_TR = 1, CNT_GY = 2, CNT_MLP = 66, CNT_SSS = 130, CNT_REC = 194, CNT_N = 258;
constexpr size_t OFF_XN = al(OFF_CNT + CNT_N * 64);
constexpr size_t OFF_WT = al(OFF_XN + (size_t)S_ * D_ * 2);
constexpr size_t OFF_GT = al(OFF_WT + (size_t)NW * D_ * 2);
constexpr size_t OFF_YT = al(OFF_GT + (size_t)S_ * HD * 2);
constexpr size_t OFF_W1T = al(OFF_YT + (size_t)S_ * HD * 2);
constexpr size_t OFF_W2T = al(OFF_W1T + (size_t)F_ * D_ * 2);
constexpr size_t OFF_MLP = al(OFF_W2T + (size_t)F_ * D_ * 2);
constexpr size_t OFF_GM = al(OFF_MLP + (size_t)S_ * D_ * 2);
constexpr size_t OFF_ZM = al(OFF_GM + (size_t)NSEG * H_ * M_ * D_ * 2);
constexpr size_t OFF_SSS = al(OFF_ZM + (size_t)NSEG * H_ * M_ * D_ * 2);
constexpr size_t WS_END = al(OFF_SSS + (size_t)NSEG * H_ * 32 * 32 * 4);

__device__ __forceinline__ float bf2f(u16 v) { return __uint_as_float(((unsigned)v) << 16); }
__device__ __forceinline__ u16 f2bf(float f) { __bf16 b = (__bf16)f; return __builtin_bit_cast(u16, b); }
__device__ __forceinline__ float rbf(float f) { return bf2f(f2bf(f)); }
__device__ __forceinline__ unsigned pk2(float a, float b) { f32x2 v = {a, b}; bf2_t r = __builtin_convertvector(v, bf2_t); return __builtin_bit_cast(unsigned, r); }
__device__ __forceinline__ float lo16(unsigned u) { return __uint_as_float(u << 16); }
__device__ __forceinline__ float hi16(unsigned u) { return __uint_as_float(u & 0xFFFF0000u); }
__device__ __forceinline__ float ex2(float x) { return __builtin_amdgcn_exp2f(x); }
__device__ __forceinline__ f32x4 mfma32(bf16x8 a, bf16x8 b, f32x4 c) { return __builtin_amdgcn_mfma_f32_16x16x32_bf16(a, b, c, 0, 0, 0); }
__device__ __forceinline__ f32x4 mfma16(bf16x4 a, bf16x4 b, f32x4 c) { return __builtin_amdgcn_mfma_f32_16x16x16bf16_1k(a, b, c, 0, 0, 0); }
__device__ __forceinline__ bf16x8 ldg8(const u16* p) { return *reinterpret_cast<const bf16x8*>(p); }
__device__ __forceinline__ u32x2 cvt4u(f32x4 v) { u32x2 r; r[0] = pk2(v[0], v[1]); r[1] = pk2(v[2], v[3]); return r; }
__device__ __forceinline__ bf16x4 cvt4(f32x4 v) { return __builtin_bit_cast(bf16x4, cvt4u(v)); }
__device__ __forceinline__ float xsum16(float x) { unsigned u = __float_as_uint(x); u32x2 r = __builtin_amdgcn_permlane16_swap(u, u, false, false); return __uint_as_float(r[0]) + __uint_as_float(r[1]); }
__device__ __forceinline__ float xsum32(float x) { unsigned u = __float_as_uint(x); u32x2 r = __builtin_amdgcn_permlane32_swap(u, u, false, false); return __uint_as_float(r[0]) + __uint_as_float(r[1]); }
__device__ __forceinline__ float xmax16(float x) { unsigned u = __float_as_uint(x); u32x2 r = __builtin_amdgcn_permlane16_swap(u, u, false, false); return fmaxf(__uint_as_float(r[0]), __uint_as_float(r[1])); }
__device__ __forceinline__ float xmax32(float x) { unsigned u = __float_as_uint(x); u32x2 r = __builtin_amdgcn_permlane32_swap(u, u, false, false); return fmaxf(__uint_as_float(r[0]), __uint_as_float(r[1])); }
__device__ __forceinline__ bool has_sent(u32x4 v) { return v[0] == SENT || v[1] == SENT || v[2] == SENT || v[3] == SENT; }
__device__ __forceinline__ unsigned canon(unsigned v) { return v == SENT ? 0x7FC07FC0u : v; }

__device__ __forceinline__ int otid() { int t = threadIdx.x; asm volatile("" : "+v"(t)); return t; }
#define LAS __attribute__((address_space(3)))
__device__ __forceinline__ void glds16_sc1(const void* gsrc, unsigned lds_dst) {
  unsigned keep;
  asm volatile("s_mov_b32 %0, m0\n\ts_mov_b32 m0, %2\n\ts_nop 0\n\tglobal_load_lds_dwordx4 %1, off sc1\n\ts_mov_b32 m0, %0" : "=&s"(keep) : "v"(gsrc), "s"(lds_dst) : "memory");
}
struct Params {
  const u16 *x, *mem0, *Wq, *Wk, *Wv, *Wo, *w1, *b1, *w2, *b2;
  u16* out;
  char* ws;
};
typedef __amdgpu_buffer_rsrc_t rsrc_t;
__device__ __forceinline__ rsrc_t ws_rsrc(const Params& p) { return __builtin_amdgcn_make_buffer_rsrc(p.ws, 0, (int)WS_END, 0x00020000); }
__device__ __forceinline__ void st64(rsrc_t rs, unsigned off, u32x2 v) { __builtin_amdgcn_raw_buffer_store_b64(v, rs, (int)off, 0, AUX_SC1); }
__device__ __forceinline__ void st128(rsrc_t rs, unsigned off, u32x4 v) { __builtin_amdgcn_raw_buffer_store_b128(v, rs, (int)off, 0, AUX_SC1); }
__device__ __forceinline__ void st32(rsrc_t rs, unsigned off, unsigned v) { __builtin_amdgcn_raw_buffer_store_b32(v, rs, (int)off, 0, AUX_SC1); }
__device__ __forceinline__ u32x4 ld128(rsrc_t rs, unsigned off) { return __builtin_amdgcn_raw_buffer_load_b128(rs, (int)off, 0, AUX_SC1); }
__device__ __forceinline__ unsigned ld32(rsrc_t rs, unsigned off) { return __builtin_amdgcn_raw_buffer_load_b32(rs, (int)off, 0, AUX_SC1); }
__device__ __forceinline__ bf16x8 ld8s(rsrc_t rs, unsigned off) { return __builtin_bit_cast(bf16x8, ld128(rs, off)); }
__device__ __forceinline__ gu32* cnt_ptr(const Params& p, int i) { return (gu32*)(p.ws + OFF_CNT + (size_t)i * 64); }
constexpr unsigned SPIN_CAP = 1u << 22;
#define SLZ 6
#define SLN 36
#define NOST 0
#define NOPF 0
#define SLW 0
#define SLI 0
#define SLX 22
#define SLY 6

__device__ __forceinline__ void signal_add(const Params& p, int cnt, unsigned v) {
  asm volatile("s_waitcnt vmcnt(0)" ::: "memory");
  __syncthreads();
  if (threadIdx.x == 0) __hip_atomic_fetch_add(cnt_ptr(p, cnt), v, __ATOMIC_RELAXED, __HIP_MEMORY_SCOPE_AGENT);
}
__device__ __forceinline__ void wait_cnt(const Params& p, int cnt, unsigned want) {
  if (threadIdx.x == 0) {
    unsigned spins = 0;
    while (__hip_atomic_load(cnt_ptr(p, cnt), __ATOMIC_RELAXED, __HIP_MEMORY_SCOPE_AGENT) < want) {
      __builtin_amdgcn_s_sleep(64);
      if (++spins > SPIN_CAP) break;
    }
  }
}
__device__ __forceinline__ void acquire_all() {
  if (threadIdx.x == 0) { __builtin_amdgcn_fence(__ATOMIC_ACQUIRE, "agent"); asm volatile("s_waitcnt vmcnt(0)" ::: "memory"); }
  __syncthreads();
}

constexpr int PLD = 136;
__global__ __launch_bounds__(448) void prep_kernel(Params p) {
  __shared__ __attribute__((aligned(16))) u16 sx[112 * PLD];
  __shared__ __attribute__((aligned(16))) u16 sy[224 * PLD];
  const int tid = threadIdx.x, lane = tid & 63, w = tid >> 6, fr = lane & 15, fq = lane >> 4;
  const int b = blockIdx.x;
  const int mat = (b & 7) * 4 + (b >> 6), tr = (b >> 4) & 3, tc = (b >> 3) & 1;
  const int h = mat & 15; const bool isU = mat >= 16;
  const u16* Yop = isU ? p.Wv : p.Wk;
  bf16x8 vx[2][4], vy[2][8];
#pragma unroll
  for (int kh = 0; kh < 2; ++kh) {
#pragma unroll
    for (int u = 0; u < 4; ++u) {
      const int i = tid + u * 448;
      if (isU) vx[kh][u] = ldg8(p.Wo + ((size_t)h * E_ + kh * 128 + i / 14) * D_ + tr * 112 + (i % 14) * 8);
      else vx[kh][u] = ldg8(p.Wq + ((size_t)(tr * 112 + (i >> 4)) * H_ + h) * E_ + kh * 128 + (i & 15) * 8);
    }
#pragma unroll
    for (int u = 0; u < 8; ++u) {
      const int i = tid + u * 448;
      vy[kh][u] = ldg8(Yop + ((size_t)(tc * 224 + (i >> 4)) * H_ + h) * E_ + kh * 128 + (i & 15) * 8);
    }
  }
  {
    constexpr unsigned N16 = (unsigned)((SZ_HAND - M_ * D_ * 2) / 16);
    u32x4 f = {SENT, SENT, SENT, SENT};
    u32x4* dst = (u32x4*)(p.ws + M_ * D_ * 2);
    for (unsigned i = b * 448 + tid; i < N16; i += 256 * 448) dst[i] = f;
  }
  if (b == 255) {
    u16* memb = (u16*)(p.ws + OFF_MEMB);
    for (int i = tid; i < M_ * D_; i += 448) { int r = i / D_, d = i % D_; memb[((d >> 3) * 8 + r) * 8 + (d & 7)] = p.mem0[i]; }
    for (int i = tid; i < CNT_N; i += 448) *(unsigned*)(p.ws + OFF_CNT + (size_t)i * 64) = 0u;
  }
  for (int rr = w; rr < 8; rr += 7) {
    const int row = b * 8 + rr;
    float v[8]; float s = 0.f;
    if (lane < 56) {
      bf16x8 xv = ldg8(p.x + (size_t)row * D_ + lane * 8);
#pragma unroll
      for (int i = 0; i < 8; ++i) { v[i] = bf2f((u16)xv[i]); s += v[i]; }
    } else {
#pragma unroll
      for (int i = 0; i < 8; ++i) v[i] = 0.f;
    }
#pragma unroll
    for (int o = 32; o > 0; o >>= 1) s += __shfl_xor(s, o);
    const float mean = s * (1.f / D_);
    float q = 0.f;
    if (lane < 56) {
#pragma unroll
      for (int i = 0; i < 8; ++i) { float d = v[i] - mean; q += d * d; }
    }
#pragma unroll
    for (int o = 32; o > 0; o >>= 1) q += __shfl_xor(q, o);
    const float var = rbf(q * (1.f / D_));
    const float mb = rbf(mean);
    const float sd = rbf(sqrtf(rbf(var + 1e-5f)));
    if (lane < 56) {
      u32x4 o;
#pragma unroll
      for (int i = 0; i < 4; ++i) o[i] = pk2(rbf(v[2 * i] - mb) / sd, rbf(v[2 * i + 1] - mb) / sd);
      *reinterpret_cast<u32x4*>((u16*)(p.ws + OFF_XN) + (size_t)row * D_ + lane * 8) = o;
    }
  }
  f32x4 acc[7][2] = {};
#pragma unroll
  for (int kh = 0; kh < 2; ++kh) {
    if (kh) __syncthreads();
#pragma unroll
    for (int u = 0; u < 4; ++u) {
      const int i = tid + u * 448;
      if (isU) {
        const int e = i / 14, c = i % 14;
#pragma unroll
        for (int j = 0; j < 8; ++j) sx[(c * 8 + j) * PLD + ((((e >> 3) ^ c) & 15) << 3) + (e & 7)] = (u16)vx[kh][u][j];
      } else *reinterpret_cast<bf16x8*>(&sx[(i >> 4) * PLD + ((((i & 15) ^ (i >> 7)) & 15) << 3)]) = vx[kh][u];
    }
#pragma unroll
    for (int u = 0; u < 8; ++u) { const int i = tid + u * 448; *reinterpret_cast<bf16x8*>(&sy[(i >> 4) * PLD + (i & 15) * 8]) = vy[kh][u]; }
    __syncthreads();
#pragma unroll
    for (int k = 0; k < 4; ++k) {
      bf16x8 yb[2];
#pragma unroll
      for (int j = 0; j < 2; ++j) yb[j] = *reinterpret_cast<const bf16x8*>(&sy[((2 * w + j) * 16 + fr) * PLD + k * 32 + fq * 8]);
#pragma unroll
      for (int i = 0; i < 7; ++i) {
        bf16x8 xa = *reinterpret_cast<const bf16x8*>(&sx[(i * 16 + fr) * PLD + ((((k * 4 + fq) ^ (i * 2 + (fr >> 3))) & 15) << 3)]);
#pragma unroll
        for (int j = 0; j < 2; ++j) acc[i][j] = mfma32(yb[j], xa, acc[i][j]);
      }
    }
  }
  u16* WT = (u16*)(p.ws + OFF_WT);
  const float sc = isU ? 1.f : 0.0625f * 1.4426950408889634f;
#pragma unroll
  for (int i = 0; i < 7; ++i)
#pragma unroll
    for (int j = 0; j < 2; ++j) {
      const int row = (isU ? HD : 0) + h * D_ + tr * 112 + i * 16 + fr;
      const int col = tc * 224 + (2 * w + j) * 16 + fq * 4;
      *reinterpret_cast<bf16x4*>(WT + (size_t)row * D_ + col) = cvt4(acc[i][j] * sc);
    }
}

constexpr int XS_LD = 456;
template <int NROWS>
__device__ __forceinline__ void stage_x_t(const Params& p, u16* xs, int row0) {
  const u16* xn = (const u16*)(p.ws + OFF_XN) + (size_t)row0 * D_;
  constexpr int N = NROWS * 56, IT = (N + 511) / 512;
  const int t = otid();
  bf16x8 v[IT];
#pragma unroll
  for (int u = 0; u < IT; ++u) { const int i = t + u * 512; if (i < N) v[u] = ldg8(xn + (size_t)i * 8); }
#pragma unroll
  for (int u = 0; u < IT; ++u) { const int i = t + u * 512; if (i < N) { const int r = i / 56, c = i % 56; *reinterpret_cast<bf16x8*>(&xs[r * XS_LD + c * 8]) = v[u]; } }
}

template <bool ISG>
__device__ __forceinline__ void producer_t(const Params& p, rsrc_t rs, char* smem, int nt) {
  const int tid = otid(), lane = tid & 63, w = tid >> 6, fr = lane & 15, fq = lane >> 4;
  u16* xs = (u16*)smem;
  u16* ot = xs + 2 * 32 * XS_LD;
  const u16* WT = (const u16*)(p.ws + OFF_WT);
  const u16* xn = (const u16*)(p.ws + OFF_XN);
  bf16x8 wb[14];
#pragma unroll
  for (int k = 0; k < 14; ++k) wb[k] = ldg8(WT + (size_t)(nt * 128 + w * 16 + fr) * D_ + k * 32 + fq * 8);
  bf16x8 xv[4];
#pragma unroll
  for (int u = 0; u < 4; ++u) { const int i = tid + u * 512; if (i < 1792) xv[u] = ldg8(xn + (size_t)i * 8); }
  for (int s = 0; s < NSEG; ++s) {
    u16* xb = xs + (s & 1) * 32 * XS_LD;
#pragma unroll
    for (int u = 0; u < 4; ++u) { const int i = tid + u * 512; if (i < 1792) { const int r = i / 56, c = i % 56; *reinterpret_cast<bf16x8*>(&xb[r * XS_LD + c * 8]) = xv[u]; } }
    asm volatile("s_waitcnt vmcnt(0)" ::: "memory");
    __syncthreads();
    if (s > 1 && tid == 0) __hip_atomic_fetch_add(cnt_ptr(p, CNT_GY + s - 1), 1u, __ATOMIC_RELAXED, __HIP_MEMORY_SCOPE_AGENT);
    if (s + 1 < NSEG) {
#pragma unroll
      for (int u = 0; u < 4; ++u) { const int i = tid + u * 512; if (i < 1792) xv[u] = ldg8(xn + (size_t)(s + 1) * 32 * D_ + (size_t)i * 8); }
    }
    f32x4 acc[2] = {};
#pragma unroll
    for (int k = 0; k < 14; ++k)
#pragma unroll
      for (int i = 0; i < 2; ++i) {
        bf16x8 xa = *reinterpret_cast<const bf16x8*>(&xb[(i * 16 + fr) * XS_LD + k * 32 + fq * 8]);
        acc[i] = ISG ? mfma32(wb[k], xa, acc[i]) : mfma32(xa, wb[k], acc[i]);
      }
    if (ISG) {
#pragma unroll
      for (int i = 0; i < 2; ++i) *reinterpret_cast<u32x2*>(&ot[(i * 16 + fr) * 136 + w * 16 + fq * 4]) = cvt4u(acc[i]);
    } else {
#pragma unroll
      for (int i = 0; i < 2; ++i) *reinterpret_cast<u32x2*>(&ot[(w * 16 + fr) * 36 + i * 16 + fq * 4]) = cvt4u(acc[i]);
    }
    __syncthreads();
    if (ISG) {
      const int r = tid >> 4, cch = tid & 15;
      const u32x2 a = *reinterpret_cast<const u32x2*>(&ot[r * 136 + cch * 8]), b2 = *reinterpret_cast<const u32x2*>(&ot[r * 136 + cch * 8 + 4]);
      u32x4 v = {a[0], a[1], b2[0], b2[1]};
      st128(rs, (unsigned)OFF_GT + (unsigned)((s * 32 + r) * HD + nt * 128 + cch * 8) * 2u, v);
    } else {
      const int row = tid >> 2, cch = tid & 3;
      const u32x2 a = *reinterpret_cast<const u32x2*>(&ot[row * 36 + cch * 8]), b2 = *reinterpret_cast<const u32x2*>(&ot[row * 36 + cch * 8 + 4]);
      u32x4 v = {a[0], a[1], b2[0], b2[1]};
      st128(rs, (unsigned)OFF_YT + (unsigned)((s * HD + nt * 128 - HD + row) * 32 + cch * 8) * 2u, v);
    }
    if (s == 0) signal_add(p, CNT_GY, 1u);
  }
  signal_add(p, CNT_GY + NSEG - 1, 1u);
}

__device__ __forceinline__ void tr_item(const Params& p, rsrc_t rs, char* smem, int it) {
  const int tid = otid();
  u16* sx = (u16*)smem;
  for (int t = it * 8; t < it * 8 + 8; ++t) {
    const bool second = t >= 196; const int tt = second ? t - 196 : t;
    const u16* src = second ? p.w2 : p.w1;
    const unsigned dst = (unsigned)(second ? OFF_W2T : OFF_W1T);
    const int R = second ? F_ : D_, C = second ? D_ : F_;
    const int nct = C / 64, rt = tt / nct, ct = tt % nct;
    __syncthreads();
    { const int r = tid >> 3, c = tid & 7;
      bf16x8 v = ldg8(src + (size_t)(rt * 64 + r) * C + ct * 64 + c * 8);
#pragma unroll
      for (int j = 0; j < 8; ++j) sx[(c * 8 + j) * 72 + r] = (u16)v[j]; }
    __syncthreads();
    { const int c = tid >> 3, r = tid & 7;
      st128(rs, dst + (unsigned)((ct * 64 + c) * R + rt * 64 + r * 8) * 2u, *reinterpret_cast<const u32x4*>(&sx[c * 72 + r * 8])); }
  }
  signal_add(p, CNT_TR, 1u);
}

constexpr int HS_LD = 1800;
__device__ __forceinline__ float gelu_tanh(float x) {
  const float u = 0.7978845608028654f * (x + 0.044715f * x * x * x);
  const float e = __expf(2.f * u);
  const float th = 1.f - 2.f / (e + 1.f);
  return 0.5f * x * (1.f + th);
}
__device__ __forceinline__ void mlp_item(const Params& p, rsrc_t rs, char* smem, int rb) {
  const int tid = otid(), lane = tid & 63, w = tid >> 6, fr = lane & 15, fq = lane >> 4;
  u16* xs = (u16*)smem;
  u16* hs = xs + 32 * XS_LD;
  wait_cnt(p, CNT_TR, 49u);
  acquire_all();
  stage_x_t<32>(p, xs, rb * 32);
  __syncthreads();
  const u16* w1T = (const u16*)(p.ws + OFF_W1T);
  for (int nt = w; nt < 112; nt += 16) {
    f32x4 acc[2][2] = {};
    bf16x8 wb[2][14];
#pragma unroll
    for (int u = 0; u < 2; ++u)
#pragma unroll
      for (int k = 0; k < 14; ++k) wb[u][k] = ldg8(w1T + (size_t)((nt + 8 * u) * 16 + fr) * D_ + k * 32 + fq * 8);
#pragma unroll
    for (int k = 0; k < 14; ++k)
#pragma unroll
      for (int i = 0; i < 2; ++i) {
        bf16x8 xa = *reinterpret_cast<const bf16x8*>(&xs[(i * 16 + fr) * XS_LD + k * 32 + fq * 8]);
#pragma unroll
        for (int u = 0; u < 2; ++u) acc[u][i] = mfma32(wb[u][k], xa, acc[u][i]);
      }
#pragma unroll
    for (int u = 0; u < 2; ++u) {
      const int n = (nt + 8 * u) * 16 + fq * 4;
      bf16x4 bv = *reinterpret_cast<const bf16x4*>(p.b1 + n);
#pragma unroll
      for (int i = 0; i < 2; ++i) {
        f32x4 g;
#pragma unroll
        for (int r = 0; r < 4; ++r) g[r] = gelu_tanh(rbf(rbf(acc[u][i][r]) + bf2f((u16)bv[r])));
        *reinterpret_cast<bf16x4*>(&hs[(i * 16 + fr) * HS_LD + n]) = cvt4(g);
      }
    }
  }
  __syncthreads();
  const u16* w2T = (const u16*)(p.ws + OFF_W2T);
  const int kh = w & 1, ng = w >> 1;
  f32x4 acc[2][7] = {};
  for (int k4 = 0; k4 < 7; ++k4) {
    bf16x8 wb[4][7];
#pragma unroll
    for (int u = 0; u < 4; ++u)
#pragma unroll
      for (int j = 0; j < 7; ++j) wb[u][j] = ldg8(w2T + (size_t)((ng * 7 + j) * 16 + fr) * F_ + kh * 896 + (k4 * 4 + u) * 32 + fq * 8);
#pragma unroll
    for (int u = 0; u < 4; ++u) {
      const int kk = kh * 896 + (k4 * 4 + u) * 32 + fq * 8;
#pragma unroll
      for (int i = 0; i < 2; ++i) {
        bf16x8 ha = *reinterpret_cast<const bf16x8*>(&hs[(i * 16 + fr) * HS_LD + kk]);
#pragma unroll
        for (int j = 0; j < 7; ++j) acc[i][j] = mfma32(wb[u][j], ha, acc[i][j]);
      }
    }
  }
  __syncthreads();
  float* red = (float*)smem;
  if (kh == 1) {
#pragma unroll
    for (int i = 0; i < 2; ++i)
#pragma unroll
      for (int j = 0; j < 7; ++j) *reinterpret_cast<f32x4*>(&red[(i * 16 + fr) * D_ + (ng * 7 + j) * 16 + fq * 4]) = acc[i][j];
  }
  __syncthreads();
  if (kh == 0) {
#pragma unroll
    for (int i = 0; i < 2; ++i)
#pragma unroll
      for (int j = 0; j < 7; ++j) {
        const int n = (ng * 7 + j) * 16 + fq * 4;
        f32x4 o = acc[i][j] + *reinterpret_cast<const f32x4*>(&red[(i * 16 + fr) * D_ + n]);
        bf16x4 bv = *reinterpret_cast<const bf16x4*>(p.b2 + n);
#pragma unroll
        for (int r = 0; r < 4; ++r) o[r] = rbf(o[r]) + bf2f((u16)bv[r]);
        st64(rs, (unsigned)OFF_MLP + (unsigned)((rb * 32 + i * 16 + fr) * D_ + n) * 2u, cvt4u(o));
      }
  }
  signal_add(p, CNT_MLP + rb, 1u);
}

__device__ __forceinline__ void sss_item(const Params& p, rsrc_t rs, int s) {
  const int tid = otid(), lane = tid & 63, w = tid >> 6, fr = lane & 15, fq = lane >> 4;
  wait_cnt(p, CNT_GY + s, 112u);
  acquire_all();
  const u16* xn = (const u16*)(p.ws + OFF_XN) + (size_t)s * 32 * D_;
  const u16* Gt = (const u16*)(p.ws + OFF_GT) + (size_t)s * 32 * HD;
  for (int hh = 0; hh < 2; ++hh) {
    const int h = w * 2 + hh;
    f32x4 acc[2][2] = {};
#pragma unroll
    for (int k = 0; k < 14; ++k) {
      bf16x8 xa[2], gb[2];
#pragma unroll
      for (int i = 0; i < 2; ++i) {
        xa[i] = ldg8(xn + (size_t)(i * 16 + fr) * D_ + k * 32 + fq * 8);
        gb[i] = ldg8(Gt + (size_t)(i * 16 + fr) * HD + h * D_ + k * 32 + fq * 8);
      }
#pragma unroll
      for (int i = 0; i < 2; ++i)
#pragma unroll
        for (int j = 0; j < 2; ++j) acc[i][j] = mfma32(gb[j], xa[i], acc[i][j]);
    }
#pragma unroll
    for (int i = 0; i < 2; ++i)
#pragma unroll
      for (int j = 0; j < 2; ++j)
        st128(rs, (unsigned)OFF_SSS + (unsigned)(((s * H_ + h) * 32 + i * 16 + fr) * 32 + j * 16 + fq * 4) * 4u, __builtin_bit_cast(u32x4, acc[i][j]));
  }
  signal_add(p, CNT_SSS + s, 1u);
}

constexpr int PS_LD = 648;
__device__ __forceinline__ void tail_item(const Params& p, rsrc_t rs, char* smem, int s, int cq) {
  const int tid = otid(), lane = tid & 63, w = tid >> 6, fr = lane & 15, fq = lane >> 4;
  float* ssm = (float*)smem;
  u16* ps = (u16*)(smem + 16 * 32 * 8 * 4);
  u16* xs = ps + 32 * PS_LD;
  wait_cnt(p, CNT_SSS + s, 1u);
  wait_cnt(p, CNT_MLP + s, 1u);
  acquire_all();
  stage_x_t<32>(p, xs, s * 32);
  const int hrow = tid >> 5, trow = tid & 31;
  f32x4 sv[8];
  {
    const float* sr = (const float*)(p.ws + OFF_SSS) + (((size_t)s * H_ + hrow) * 32 + trow) * 32;
#pragma unroll
    for (int k = 0; k < 8; ++k) sv[k] = *reinterpret_cast<const f32x4*>(sr + k * 4);
  }
  bf16x8 wb[20];
  const int dcol = cq * 112 + (w < 7 ? w : 0) * 16 + fr;
  {
    const u16* Yt = (const u16*)(p.ws + OFF_YT) + (size_t)s * HD * 32;
#pragma unroll
    for (int k = 0; k < 16; ++k) wb[k] = ldg8(Yt + ((size_t)k * D_ + dcol) * 32 + fq * 8);
  }
  f32x4 base[2];
  {
    const u16* mo = (const u16*)(p.ws + OFF_MLP);
#pragma unroll
    for (int i = 0; i < 2; ++i) {
      const size_t o = (size_t)(s * 32 + i * 16 + fr) * D_ + cq * 112 + (w < 7 ? w : 0) * 16 + fq * 4;
      u32x2 xv = *reinterpret_cast<const u32x2*>(p.x + o), bv = *reinterpret_cast<const u32x2*>(mo + o);
      base[i][0] = __uint_as_float(xv[0]); base[i][1] = __uint_as_float(xv[1]); base[i][2] = __uint_as_float(bv[0]); base[i][3] = __uint_as_float(bv[1]);
    }
  }
  if (tid == 0) {
    unsigned spins = 0;
    if (s < NSEG - 1) {
      while (__hip_atomic_load(cnt_ptr(p, CNT_REC + s), __ATOMIC_RELAXED, __HIP_MEMORY_SCOPE_AGENT) < 64u) { __builtin_amdgcn_s_sleep(127); if (++spins > SPIN_CAP) break; }
    } else {
      while (__hip_atomic_load(cnt_ptr(p, CNT_REC + s - 1), __ATOMIC_RELAXED, __HIP_MEMORY_SCOPE_AGENT) < 64u) { __builtin_amdgcn_s_sleep(127); if (++spins > SPIN_CAP) break; }
      spins = 0;
      while (__hip_atomic_load(cnt_ptr(p, CNT_REC + s), __ATOMIC_RELAXED, __HIP_MEMORY_SCOPE_AGENT) < 64u) { __builtin_amdgcn_s_sleep(2); if (++spins > SPIN_CAP) break; }
    }
  }
  __syncthreads();
  {
    bf16x8 gb[14];
#pragma unroll
    for (int k = 0; k < 14; ++k) {
      const int d = k * 32 + fq * 8, hh2 = w * 2 + (fr >> 3), jq2 = d / 112, dd = d - jq2 * 112;
      gb[k] = ld8s(rs, (unsigned)OFF_GM + (unsigned)(s * (H_ * M_ * D_) + (((hh2 * 4 + jq2) * 7 + (dd >> 4)) * 8 + (fr & 7)) * 16 + (dd & 15)) * 2u);
    }
#pragma unroll
    for (int k = 16; k < 20; ++k) {
      const int hz = (k - 16) * 4 + fq;
      wb[k] = ld8s(rs, (unsigned)OFF_ZM + (unsigned)(s * (H_ * D_ * M_) + (((dcol >> 3) * 8 + (hz >> 1)) * 16 + (hz & 1) * 8 + (dcol & 7)) * 8) * 2u);
    }
    f32x4 acc[2] = {};
#pragma unroll
    for (int k = 0; k < 14; ++k)
#pragma unroll
      for (int i = 0; i < 2; ++i) {
        bf16x8 xa = *reinterpret_cast<const bf16x8*>(&xs[(i * 16 + fr) * XS_LD + k * 32 + fq * 8]);
        acc[i] = mfma32(gb[k], xa, acc[i]);
      }
#pragma unroll
    for (int i = 0; i < 2; ++i)
      *reinterpret_cast<f32x4*>(&ssm[((w * 2 + (fq >> 1)) * 32 + i * 16 + fr) * 8 + (fq & 1) * 4]) = acc[i];
  }
  __syncthreads();
  {
    const int h = hrow, t = trow;
    float a[32], m[8];
#pragma unroll
    for (int k = 0; k < 8; ++k) { a[4 * k] = sv[k][0]; a[4 * k + 1] = sv[k][1]; a[4 * k + 2] = sv[k][2]; a[4 * k + 3] = sv[k][3]; }
#pragma unroll
    for (int j = 0; j < 8; ++j) m[j] = ssm[(h * 32 + t) * 8 + j];
    float mx = m[0];
#pragma unroll
    for (int j = 1; j < 8; ++j) mx = fmaxf(mx, m[j]);
#pragma unroll
    for (int k = 0; k < 32; ++k) if (k <= t) mx = fmaxf(mx, a[k]);
    float sum = 0.f;
#pragma unroll
    for (int j = 0; j < 8; ++j) { m[j] = ex2(m[j] - mx); sum += m[j]; }
#pragma unroll
    for (int k = 0; k < 32; ++k) { a[k] = (k <= t) ? ex2(a[k] - mx) : 0.f; sum += a[k]; }
    const float inv = 1.f / sum;
#pragma unroll
    for (int k = 0; k < 4; ++k) {
      u32x4 v;
#pragma unroll
      for (int r = 0; r < 4; ++r) v[r] = pk2(a[8 * k + 2 * r] * inv, a[8 * k + 2 * r + 1] * inv);
      *reinterpret_cast<u32x4*>(&ps[t * PS_LD + h * 32 + k * 8]) = v;
    }
    u32x4 v;
#pragma unroll
    for (int r = 0; r < 4; ++r) v[r] = pk2(m[2 * r] * inv, m[2 * r + 1] * inv);
    *reinterpret_cast<u32x4*>(&ps[t * PS_LD + 512 + h * 8]) = v;
  }
  __syncthreads();
  if (w < 7) {
    f32x4 acc[2] = {};
#pragma unroll
    for (int k = 0; k < 20; ++k) {
#pragma unroll
      for (int i = 0; i < 2; ++i) {
        bf16x8 pa = *reinterpret_cast<const bf16x8*>(&ps[(i * 16 + fr) * PS_LD + k * 32 + fq * 8]);
        acc[i] = mfma32(wb[k], pa, acc[i]);
      }
    }
#pragma unroll
    for (int i = 0; i < 2; ++i) {
      const size_t o = (size_t)(s * 32 + i * 16 + fr) * D_ + cq * 112 + w * 16 + fq * 4;
      const unsigned x0 = __float_as_uint(base[i][0]), x1 = __float_as_uint(base[i][1]), b0 = __float_as_uint(base[i][2]), b1 = __float_as_uint(base[i][3]);
      f32x4 r;
      r[0] = rbf(lo16(x0) + rbf(acc[i][0])) + lo16(b0);
      r[1] = rbf(hi16(x0) + rbf(acc[i][1])) + hi16(b0);
      r[2] = rbf(lo16(x1) + rbf(acc[i][2])) + lo16(b1);
      r[3] = rbf(hi16(x1) + rbf(acc[i][3])) + hi16(b1);
      *reinterpret_cast<bf16x4*>(p.out + o) = cvt4(r);
    }
  }
  __syncthreads();
}

constexpr int IT_TR = 49, IT_MLP = 113, IT_SSS = 177, IT_END = 433;
__device__ __forceinline__ void worker(const Params& p, char* smem) {
  const rsrc_t rs = ws_rsrc(p);
  unsigned* sitem = (unsigned*)(smem + 152 * 1024 - 16);
  if (blockIdx.x < 64 + 112) {
    const int nt = blockIdx.x - 64;
    if (nt < 56) producer_t<true>(p, rs, smem, nt); else producer_t<false>(p, rs, smem, nt);
  }
  for (;;) {
    __syncthreads();
    if (threadIdx.x == 0) *sitem = __hip_atomic_fetch_add(cnt_ptr(p, CNT_Q), 1u, __ATOMIC_RELAXED, __HIP_MEMORY_SCOPE_AGENT);
    __syncthreads();
    const int it = (int)*sitem;
    if (it >= IT_END) break;
    if (it < IT_TR) tr_item(p, rs, smem, it);
    else if (it < IT_MLP) mlp_item(p, rs, smem, it - IT_TR);
    else if (it < IT_SSS) sss_item(p, rs, it - IT_MLP);
    else { const int q = it - IT_SSS; tail_item(p, rs, smem, q >> 2, q & 3); }
  }
}

__device__ __forceinline__ void recurrence(const Params& p, char* smem) {
  u16* ms = (u16*)smem;
  float* red1 = (float*)(smem + 7168);
  float* red2 = red1 + 7 * 64;
  u16* zs = (u16*)(red2 + 16 * 64);
  u32x4* gbuf = (u32x4*)(smem + 16384);
  u32x4* ybuf = (u32x4*)(smem + 16384 + 28672);
  const unsigned lds0 = __builtin_amdgcn_readfirstlane((unsigned)(size_t)(LAS char*)smem);
  const int tid = threadIdx.x, lane = tid & 63, w = tid >> 6, fr = lane & 15, fq = lane >> 4;
  const int c = blockIdx.x, h = c >> 2, jq = c & 3;
  const bool isval = c < 56;
  const u16* WT = (const u16*)(p.ws + OFF_WT);
  const rsrc_t rs = ws_rsrc(p);
  const unsigned g_off = (unsigned)OFF_GT + (unsigned)((jq * 8 + (fr & 7)) * HD + h * D_ + fq * 8) * 2u;
  const int zh = 2 * w + (fr >> 3), zd = (isval ? 8 * c : 0) + (fr & 7);

  bf16x8 wa[14], wz[14];
  if (w < 7) {
#pragma unroll
    for (int k = 0; k < 14; ++k) wa[k] = ldg8(WT + (size_t)(h * D_ + jq * 112 + w * 16 + fr) * D_ + k * 32 + fq * 8);
  }
#pragma unroll
  for (int k = 0; k < 14; ++k) wz[k] = ldg8(WT + (size_t)(HD + zh * D_ + zd) * D_ + k * 32 + fq * 8);
  auto prefetch = [&](int sg) {
    const char* gp = p.ws + g_off + (size_t)sg * (32 * HD * 2);
#pragma unroll
    for (int k = 0; k < 14; ++k) glds16_sc1(gp + k * 64, lds0 + 16384 + (sg & 1) * 14336 + k * 1024);
    if (isval) {
      const char* yp = p.ws + OFF_YT + (size_t)(((fr >> 3) * D_ + zd) * 32 + fq * 8) * 2 + (size_t)sg * (HD * 32 * 2);
#pragma unroll
      for (int k = 0; k < 8; ++k) glds16_sc1(yp + (size_t)k * (2 * D_ * 32 * 2), lds0 + 16384 + 28672 + (sg & 1) * 8192 + k * 1024);
    }
  };
  if (w == 7) {
    unsigned spins = 0;
    while (__hip_atomic_load(cnt_ptr(p, CNT_GY), __ATOMIC_RELAXED, __HIP_MEMORY_SCOPE_AGENT) < 112u) { __builtin_amdgcn_s_sleep(2); if (++spins > SPIN_CAP) break; }
    prefetch(0);
    spins = 0;
    while (__hip_atomic_load(cnt_ptr(p, CNT_GY + 1), __ATOMIC_RELAXED, __HIP_MEMORY_SCOPE_AGENT) < 112u) { __builtin_amdgcn_s_sleep(2); if (++spins > SPIN_CAP) break; }
  }
  __builtin_amdgcn_s_waitcnt(0);
  for (int s = 0; s < NSEG; ++s) {
    if (w < 7) {
      const int id = w * 64 + lane, cc = id >> 3, i = id & 7;
      const unsigned off = (unsigned)OFF_MEMB + s * (M_ * D_ * 2) + id * 16;
      u32x4 v; unsigned spins = 0;
      if (s > 0) { if (!isval) __builtin_amdgcn_s_sleep(SLN); __builtin_amdgcn_s_sleep(SLX); }
      for (;;) {
        asm volatile("" ::: "memory");
        v = ld128(rs, off);
        if (!__any(has_sent(v))) break;
        if (++spins > SPIN_CAP) break;
        if (SLI > 0) __builtin_amdgcn_s_sleep(SLI);
      }
      *reinterpret_cast<u32x4*>(&ms[((cc >> 3) * 64 + (cc & 3) * 16 + ((cc >> 2) & 1) * 8 + i) * 8]) = v;
    } else {
      asm volatile("s_waitcnt vmcnt(0)" ::: "memory");
      if (s + 1 < NSEG) prefetch(s + 1);
#pragma unroll
      for (int k = 0; k < 14; ++k) wa[k] = __builtin_bit_cast(bf16x8, gbuf[((s & 1) * 14 + k) * 64 + lane]);
    }
    __syncthreads();
    bf16x8 mf[14];
#pragma unroll
    for (int k = 0; k < 7; ++k) mf[2 * k] = *reinterpret_cast<const bf16x8*>(&ms[(k * 64 + lane) * 8]);
    bf16x4 m4;
    { const int d0 = jq * 112 + (w < 7 ? w : 0) * 16 + fq * 4, cc = d0 >> 3;
      m4 = *reinterpret_cast<const bf16x4*>(&ms[((cc >> 3) * 64 + (cc & 3) * 16 + ((cc >> 2) & 1) * 8 + (fr & 7)) * 8 + (d0 & 4)]); }
    __builtin_amdgcn_sched_barrier(0);
#pragma unroll
    for (int k = 0; k < 7; ++k) {
      u32x4 t = __builtin_bit_cast(u32x4, mf[2 * k]), r;
#pragma unroll
      for (int e = 0; e < 4; ++e) r[e] = __builtin_amdgcn_mov_dpp(t[e], 0x128  , 0xF, 0xF, false);
      mf[2 * k + 1] = __builtin_bit_cast(bf16x8, r);
    }
    f32x4 acc = {}, acc1 = {};
#pragma unroll
    for (int k = 0; k < 7; ++k) { acc = mfma32(wa[2 * k], mf[2 * k], acc); acc1 = mfma32(wa[2 * k + 1], mf[2 * k + 1], acc1); }
    acc += acc1;
    u32x2 gbu = cvt4u(acc);
    bf16x4 gb = __builtin_bit_cast(bf16x4, gbu);
    if (w < 7) {
      f32x4 z4 = {};
      f32x4 sp = mfma16(gb, m4, z4);
      if (fr < 8 && fq < 2) *reinterpret_cast<f32x4*>(&red1[w * 64 + fr * 8 + fq * 4]) = sp;
      if (fr < 8 && !NOST) st64(rs, (unsigned)OFF_GM + (unsigned)((((s * 64 + c) * 7 + w) * 8 + fr) * 16 + fq * 4) * 2u, gbu);
    } else {
      if (fr < 8 && fq < 2) {
        u32x2 o; o[0] = canon(gbu[0]); o[1] = canon(gbu[1]);
        st64(rs, (unsigned)OFF_SB + s * SREC + c * 256 + fr * 16 + fq * 8, o);
      }
    }
    __syncthreads();
    if (w == 0 && lane < 32) {
      f32x2 t = *reinterpret_cast<const f32x2*>(&red1[lane * 2]);
#pragma unroll
      for (int k = 1; k < 7; ++k) t += *reinterpret_cast<const f32x2*>(&red1[k * 64 + lane * 2]);
      st32(rs, (unsigned)OFF_SB + s * SREC + c * 256 + 128 + lane * 4, canon(pk2(t[0], t[1])));
    }
    if (s > 0 && tid == 64) __hip_atomic_fetch_add(cnt_ptr(p, CNT_REC + s - 1), 1u, __ATOMIC_RELAXED, __HIP_MEMORY_SCOPE_AGENT);
    const bool need_gy = (s + 2) < NSEG;
    if (isval) {
      f32x4 az = {};
#pragma unroll
      for (int k = 0; k < 14; ++k) az = mfma32(mf[k], wz[k], az);
      u32x2 zbu = cvt4u(az);
      if (fq < 2) {
        if (!NOST) st64(rs, (unsigned)OFF_ZM + (unsigned)((((s * 56 + c) * 8 + w) * 16 + fr) * 8 + fq * 4) * 2u, zbu);
        *reinterpret_cast<u32x2*>(&zs[(w * 16 + fr) * 8 + fq * 4]) = zbu;
      }
      const u32x4 yfu = ybuf[((s & 1) * 8 + w) * 64 + lane];
      u32x4 zf = {0u, 0u, 0u, 0u};
      zf[0] = *reinterpret_cast<const unsigned*>(&zs[(w * 16 + fr) * 8 + fq * 2]);
      const int hh = fr >> 3, i = fr & 7, hd = 2 * w + hh;
      const unsigned o1 = (unsigned)OFF_SB + s * SREC + (hd * 4 + fq) * 256 + i * 16;
      const unsigned o2 = (unsigned)OFF_SB + s * SREC + hd * 1024 + 128 + i * 16 + fq * 4;
      const unsigned o3 = (unsigned)OFF_CNT + (unsigned)(CNT_GY + (need_gy ? (s + 2) : 0)) * 64u;
      u32x4 a0; unsigned b0, b1, b2, b3, gc; unsigned spins = 0;
      if (w < 4) __builtin_amdgcn_s_sleep(SLY); else __builtin_amdgcn_s_sleep(SLZ);
      for (;;) {
        asm volatile("" ::: "memory");
        a0 = ld128(rs, o1);
        b0 = ld32(rs, o2); b1 = ld32(rs, o2 + 256); b2 = ld32(rs, o2 + 512); b3 = ld32(rs, o2 + 768);
        gc = ld32(rs, o3);
        if (!__any(has_sent(a0) || b0 == SENT || b1 == SENT || b2 == SENT || b3 == SENT || gc < 112u)) break;
        if (++spins > SPIN_CAP) break;
        if (SLI > 0) __builtin_amdgcn_s_sleep(SLI);
      }
      float sg[8];
#pragma unroll
      for (int r = 0; r < 4; ++r) { sg[2 * r] = lo16(a0[r]); sg[2 * r + 1] = hi16(a0[r]); }
      float m0 = (lo16(b0) + lo16(b1)) + (lo16(b2) + lo16(b3));
      float m1 = (hi16(b0) + hi16(b1)) + (hi16(b2) + hi16(b3));
      float mx = fmaxf(fmaxf(fmaxf(sg[0], sg[1]), fmaxf(sg[2], sg[3])), fmaxf(fmaxf(sg[4], sg[5]), fmaxf(sg[6], sg[7])));
      mx = fmaxf(mx, fmaxf(m0, m1));
      mx = xmax32(xmax16(mx));
      m0 = ex2(m0 - mx); m1 = ex2(m1 - mx);
      float ss = m0 + m1;
#pragma unroll
      for (int r = 0; r < 8; ++r) { sg[r] = ex2(sg[r] - mx); ss += sg[r]; }
      ss = xsum32(xsum16(ss));
      const float inv = __builtin_amdgcn_rcpf(ss);
      u32x4 pa, pm = {0u, 0u, 0u, 0u};
#pragma unroll
      for (int r = 0; r < 4; ++r) pa[r] = pk2(sg[2 * r] * inv, sg[2 * r + 1] * inv);
      pm[0] = pk2(m0 * inv, m1 * inv);
      f32x4 o = {};
      o = mfma32(__builtin_bit_cast(bf16x8, yfu), __builtin_bit_cast(bf16x8, pa), o);
      o = mfma32(__builtin_bit_cast(bf16x8, zf), __builtin_bit_cast(bf16x8, pm), o);
      if ((fr >> 3) == (fq >> 1)) *reinterpret_cast<f32x4*>(&red2[(w * 2 + (fq >> 1)) * 64 + (fr & 7) * 8 + (fq & 1) * 4]) = o;
      __syncthreads();
      if (w == 0) {
        const int hb = (lane >> 5) * 8, l2 = (lane & 31) * 2;
        f32x2 t = *reinterpret_cast<const f32x2*>(&red2[hb * 64 + l2]);
#pragma unroll
        for (int k = 1; k < 8; ++k) t += *reinterpret_cast<const f32x2*>(&red2[(hb + k) * 64 + l2]);
        const float t0 = xsum32(t[0]), t1 = xsum32(t[1]);
        if (lane < 32) st32(rs, (unsigned)OFF_MEMB + (s + 1) * (M_ * D_ * 2) + c * 128 + lane * 4, canon(pk2(t0, t1)));
      }
    } else if (need_gy) {
      unsigned spins = 0;
      while (__hip_atomic_load(cnt_ptr(p, CNT_GY + s + 2), __ATOMIC_RELAXED, __HIP_MEMORY_SCOPE_AGENT) < 112u) { __builtin_amdgcn_s_sleep(2); if (++spins > SPIN_CAP) break; }
    }
  }
  signal_add(p, CNT_REC + NSEG - 1, 1u);
}

constexpr int LDS_MAIN = 152 * 1024;
__global__ __launch_bounds__(512, 2) void main_kernel(Params p) {
  extern __shared__ __attribute__((aligned(16))) char smem[];
  if (blockIdx.x < 64) recurrence(p, smem);
  else worker(p, smem);
}

extern "C" void kernel_launch(void* const* d_in, const int* in_sizes, int n_in, void* d_out, int out_size, void* d_ws,
                              size_t ws_size, hipStream_t stream) {
  Params p;
  p.x = (const u16*)d_in[0]; p.mem0 = (const u16*)d_in[1]; p.Wq = (const u16*)d_in[2]; p.Wk = (const u16*)d_in[3];
  p.Wv = (const u16*)d_in[4]; p.Wo = (const u16*)d_in[5]; p.w1 = (const u16*)d_in[8]; p.b1 = (const u16*)d_in[9];
  p.w2 = (const u16*)d_in[10]; p.b2 = (const u16*)d_in[11];
  p.out = (u16*)d_out; p.ws = (char*)d_ws;
  (void)hipFuncSetAttribute((const void*)main_kernel, hipFuncAttributeMaxDynamicSharedMemorySize, LDS_MAIN);
  prep_kernel<<<256, 448, 0, stream>>>(p);
  main_kernel<<<256, 512, LDS_MAIN, stream>>>(p);
}
```

```cpp
#include <hip/hip_runtime.h>
#include <hip/hip_bf16.h>
#include <stdint.h>

typedef __attribute__((ext_vector_type(8))) short bf16x8;
typedef __attribute__((ext_vector_type(4))) short bf16x4;
typedef __attribute__((ext_vector_type(4))) float f32x4;
typedef __attribute__((ext_vector_type(2))) float f32x2;
typedef __attribute__((ext_vector_type(4))) unsigned u32x4;
typedef __attribute__((ext_vector_type(2))) unsigned u32x2;
typedef __attribute__((ext_vector_type(2))) __bf16 bf2_t;
typedef unsigned short u16;
typedef __attribute__((address_space(1))) unsigned gu32;

constexpr int S_ = 2048, D_ = 448, H_ = 16, E_ = 256, M_ = 8, NSEG = 64, F_ = 1792;
constexpr int HD = H_ * D_;
constexpr int NW = 2 * HD;
constexpr int AUX_SC1 = 16;
constexpr unsigned SENT = 0xFFFFFFFFu;

constexpr size_t al(size_t x) { return (x + 255) & ~(size_t)255; }
constexpr size_t OFF_MEMB = 0;
constexpr size_t SZ_MEMB = (size_t)(NSEG + 1) * M_ * D_ * 2;
constexpr int SREC = 16384;
constexpr size_t OFF_SB = al(OFF_MEMB + SZ_MEMB);
constexpr size_t SZ_SB = (size_t)NSEG * SREC;
constexpr size_t SZ_HAND = al(OFF_SB + SZ_SB);
constexpr size_t OFF_CNT = SZ_HAND;
constexpr int CNT_Q = 0, CNT_TR = 1, CNT_GY = 2, CNT_MLP = 66, CNT_SSS = 130, CNT_REC = 194, CNT_N = 258;
constexpr size_t OFF_XN = al(OFF_CNT + CNT_N * 64);
constexpr size_t OFF_WT = al(OFF_XN + (size_t)S_ * D_ * 2);
constexpr size_t OFF_GT = al(OFF_WT + (size_t)NW * D_ * 2);
constexpr size_t OFF_YT = al(OFF_GT + (size_t)S_ * HD * 2);
constexpr size_t OFF_W1T = al(OFF_YT + (size_t)S_ * HD * 2);
constexpr size_t OFF_W2T = al(OFF_W1T + (size_t)F_ * D_ * 2);
constexpr size_t OFF_MLP = al(OFF_W2T + (size_t)F_ * D_ * 2);
constexpr size_t OFF_GM = al(OFF_MLP + (size_t)S_ * D_ * 2);
constexpr size_t OFF_ZM = al(OFF_GM + (size_t)NSEG * H_ * M_ * D_ * 2);
constexpr size_t OFF_SSS = al(OFF_ZM + (size_t)NSEG * H_ * M_ * D_ * 2);
constexpr size_t WS_END = al(OFF_SSS + (size_t)NSEG * H_ * 32 * 32 * 4);

__device__ __forceinline__ float bf2f(u16 v) { return __uint_as_float(((unsigned)v) << 16); }
__device__ __forceinline__ u16 f2bf(float f) { __bf16 b = (__bf16)f; return __builtin_bit_cast(u16, b); }
__device__ __forceinline__ float rbf(float f) { return bf2f(f2bf(f)); }
__device__ __forceinline__ unsigned pk2(float a, float b) { f32x2 v = {a, b}; bf2_t r = __builtin_convertvector(v, bf2_t); return __builtin_bit_cast(unsigned, r); }
__device__ __forceinline__ float lo16(unsigned u) { return __uint_as_float(u << 16); }
__device__ __forceinline__ float hi16(unsigned u) { return __uint_as_float(u & 0xFFFF0000u); }
__device__ __forceinline__ float ex2(float x) { return __builtin_amdgcn_exp2f(x); }
__device__ __forceinline__ f32x4 mfma32(bf16x8 a, bf16x8 b, f32x4 c) { return __builtin_amdgcn_mfma_f32_16x16x32_bf16(a, b, c, 0, 0, 0); }
__device__ __forceinline__ f32x4 mfma16(bf16x4 a, bf16x4 b, f32x4 c) { return __builtin_amdgcn_mfma_f32_16x16x16bf16_1k(a, b, c, 0, 0, 0); }
__device__ __forceinline__ bf16x8 ldg8(const u16* p) { return *reinterpret_cast<const bf16x8*>(p); }
__device__ __forceinline__ u32x2 cvt4u(f32x4 v) { u32x2 r; r[0] = pk2(v[0], v[1]); r[1] = pk2(v[2], v[3]); return r; }
__device__ __forceinline__ bf16x4 cvt4(f32x4 v) { return __builtin_bit_cast(bf16x4, cvt4u(v)); }
__device__ __forceinline__ float xsum16(float x) { unsigned u = __float_as_uint(x); u32x2 r = __builtin_amdgcn_permlane16_swap(u, u, false, false); return __uint_as_float(r[0]) + __uint_as_float(r[1]); }
__device__ __forceinline__ float xsum32(float x) { unsigned u = __float_as_uint(x); u32x2 r = __builtin_amdgcn_permlane32_swap(u, u, false, false); return __uint_as_float(r[0]) + __uint_as_float(r[1]); }
__device__ __forceinline__ float xmax16(float x) { unsigned u = __float_as_uint(x); u32x2 r = __builtin_amdgcn_permlane16_swap(u, u, false, false); return fmaxf(__uint_as_float(r[0]), __uint_as_float(r[1])); }
__device__ __forceinline__ float xmax32(float x) { unsigned u = __float_as_uint(x); u32x2 r = __builtin_amdgcn_permlane32_swap(u, u, false, false); return fmaxf(__uint_as_float(r[0]), __uint_as_float(r[1])); }
__device__ __forceinline__ bool has_sent(u32x4 v) { return v[0] == SENT || v[1] == SENT || v[2] == SENT || v[3] == SENT; }
__device__ __forceinline__ unsigned canon(unsigned v) { return v == SENT ? 0x7FC07FC0u : v; }

__device__ __forceinline__ int otid() { int t = threadIdx.x; asm volatile("" : "+v"(t)); return t; }
#define LAS __attribute__((address_space(3)))
__device__ __forceinline__ void glds16_sc1(const void* gsrc, unsigned lds_dst) {
  unsigned keep;
  asm volatile("s_mov_b32 %0, m0\n\ts_mov_b32 m0, %2\n\ts_nop 0\n\tglobal_load_lds_dwordx4 %1, off sc1\n\ts_mov_b32 m0, %0" : "=&s"(keep) : "v"(gsrc), "s"(lds_dst) : "memory");
}
struct Params {
  const u16 *x, *mem0, *Wq, *Wk, *Wv, *Wo, *w1, *b1, *w2, *b2;
  u16* out;
  char* ws;
};
typedef __amdgpu_buffer_rsrc_t rsrc_t;
__device__ __forceinline__ rsrc_t ws_rsrc(const Params& p) { return __builtin_amdgcn_make_buffer_rsrc(p.ws, 0, (int)WS_END, 0x00020000); }
__device__ __forceinline__ void st64(rsrc_t rs, unsigned off, u32x2 v) { __builtin_amdgcn_raw_buffer_store_b64(v, rs, (int)off, 0, AUX_SC1); }
__device__ __forceinline__ void st128(rsrc_t rs, unsigned off, u32x4 v) { __builtin_amdgcn_raw_buffer_store_b128(v, rs, (int)off, 0, AUX_SC1); }
__device__ __forceinline__ void st32(rsrc_t rs, unsigned off, unsigned v) { __builtin_amdgcn_raw_buffer_store_b32(v, rs, (int)off, 0, AUX_SC1); }
__device__ __forceinline__ u32x4 ld128(rsrc_t rs, unsigned off) { return __builtin_amdgcn_raw_buffer_load_b128(rs, (int)off, 0, AUX_SC1); }
__device__ __forceinline__ unsigned ld32(rsrc_t rs, unsigned off) { return __builtin_amdgcn_raw_buffer_load_b32(rs, (int)off, 0, AUX_SC1); }
__device__ __forceinline__ bf16x8 ld8s(rsrc_t rs, unsigned off) { return __builtin_bit_cast(bf16x8, ld128(rs, off)); }
__device__ __forceinline__ gu32* cnt_ptr(const Params& p, int i) { return (gu32*)(p.ws + OFF_CNT + (size_t)i * 64); }
constexpr unsigned SPIN_CAP = 1u << 22;
constexpr int SLX = 22;
constexpr int SLN = 36;
constexpr int SLY = 6;

__device__ __forceinline__ void signal_add(const Params& p, int cnt, unsigned v) {
  asm volatile("s_waitcnt vmcnt(0)" ::: "memory");
  __syncthreads();
  if (threadIdx.x == 0) __hip_atomic_fetch_add(cnt_ptr(p, cnt), v, __ATOMIC_RELAXED, __HIP_MEMORY_SCOPE_AGENT);
}
__device__ __forceinline__ void wait_cnt(const Params& p, int cnt, unsigned want) {
  if (threadIdx.x == 0) {
    unsigned spins = 0;
    while (__hip_atomic_load(cnt_ptr(p, cnt), __ATOMIC_RELAXED, __HIP_MEMORY_SCOPE_AGENT) < want) {
      __builtin_amdgcn_s_sleep(64);
      if (++spins > SPIN_CAP) break;
    }
  }
}
__device__ __forceinline__ void acquire_all() {
  if (threadIdx.x == 0) { __builtin_amdgcn_fence(__ATOMIC_ACQUIRE, "agent"); asm volatile("s_waitcnt vmcnt(0)" ::: "memory"); }
  __syncthreads();
}

constexpr int PLD = 136;
__global__ __launch_bounds__(448) void prep_kernel(Params p) {
  __shared__ __attribute__((aligned(16))) u16 sx[112 * PLD];
  __shared__ __attribute__((aligned(16))) u16 sy[224 * PLD];
  const int tid = threadIdx.x, lane = tid & 63, w = tid >> 6, fr = lane & 15, fq = lane >> 4;
  const int b = blockIdx.x;
  const int mat = (b & 7) * 4 + (b >> 6), tr = (b >> 4) & 3, tc = (b >> 3) & 1;
  const int h = mat & 15; const bool isU = mat >= 16;
  const u16* Yop = isU ? p.Wv : p.Wk;
  bf16x8 vx[2][4], vy[2][8];
#pragma unroll
  for (int kh = 0; kh < 2; ++kh) {
#pragma unroll
    for (int u = 0; u < 4; ++u) {
      const int i = tid + u * 448;
      if (isU) vx[kh][u] = ldg8(p.Wo + ((size_t)h * E_ + kh * 128 + i / 14) * D_ + tr * 112 + (i % 14) * 8);
      else vx[kh][u] = ldg8(p.Wq + ((size_t)(tr * 112 + (i >> 4)) * H_ + h) * E_ + kh * 128 + (i & 15) * 8);
    }
#pragma unroll
    for (int u = 0; u < 8; ++u) {
      const int i = tid + u * 448;
      vy[kh][u] = ldg8(Yop + ((size_t)(tc * 224 + (i >> 4)) * H_ + h) * E_ + kh * 128 + (i & 15) * 8);
    }
  }
  {
    constexpr unsigned N16 = (unsigned)((SZ_HAND - M_ * D_ * 2) / 16);
    u32x4 f = {SENT, SENT, SENT, SENT};
    u32x4* dst = (u32x4*)(p.ws + M_ * D_ * 2);
    for (unsigned i = b * 448 + tid; i < N16; i += 256 * 448) dst[i] = f;
  }
  if (b == 255) {
    u16* memb = (u16*)(p.ws + OFF_MEMB);
    for (int i = tid; i < M_ * D_; i += 448) { int r = i / D_, d = i % D_; memb[((d >> 3) * 8 + r) * 8 + (d & 7)] = p.mem0[i]; }
    for (int i = tid; i < CNT_N; i += 448) *(unsigned*)(p.ws + OFF_CNT + (size_t)i * 64) = 0u;
  }
  for (int rr = w; rr < 8; rr += 7) {
    const int row = b * 8 + rr;
    float v[8]; float s = 0.f;
    if (lane < 56) {
      bf16x8 xv = ldg8(p.x + (size_t)row * D_ + lane * 8);
#pragma unroll
      for (int i = 0; i < 8; ++i) { v[i] = bf2f((u16)xv[i]); s += v[i]; }
    } else {
#pragma unroll
      for (int i = 0; i < 8; ++i) v[i] = 0.f;
    }
#pragma unroll
    for (int o = 32; o > 0; o >>= 1) s += __shfl_xor(s, o);
    const float mean = s * (1.f / D_);
    float q = 0.f;
    if (lane < 56) {
#pragma unroll
      for (int i = 0; i < 8; ++i) { float d = v[i] - mean; q += d * d; }
    }
#pragma unroll
    for (int o = 32; o > 0; o >>= 1) q += __shfl_xor(q, o);
    const float var = rbf(q * (1.f / D_));
    const float mb = rbf(mean);
    const float sd = rbf(sqrtf(rbf(var + 1e-5f)));
    if (lane < 56) {
      u32x4 o;
#pragma unroll
      for (int i = 0; i < 4; ++i) o[i] = pk2(rbf(v[2 * i] - mb) / sd, rbf(v[2 * i + 1] - mb) / sd);
      *reinterpret_cast<u32x4*>((u16*)(p.ws + OFF_XN) + (size_t)row * D_ + lane * 8) = o;
    }
  }
  f32x4 acc[7][2] = {};
#pragma unroll
  for (int kh = 0; kh < 2; ++kh) {
    if (kh) __syncthreads();
#pragma unroll
    for (int u = 0; u < 4; ++u) {
      const int i = tid + u * 448;
      if (isU) {
        const int e = i / 14, c = i % 14;
#pragma unroll
        for (int j = 0; j < 8; ++j) sx[(c * 8 + j) * PLD + ((((e >> 3) ^ c) & 15) << 3) + (e & 7)] = (u16)vx[kh][u][j];
      } else *reinterpret_cast<bf16x8*>(&sx[(i >> 4) * PLD + ((((i & 15) ^ (i >> 7)) & 15) << 3)]) = vx[kh][u];
    }
#pragma unroll
    for (int u = 0; u < 8; ++u) { const int i = tid + u * 448; *reinterpret_cast<bf16x8*>(&sy[(i >> 4) * PLD + (i & 15) * 8]) = vy[kh][u]; }
    __syncthreads();
#pragma unroll
    for (int k = 0; k < 4; ++k) {
      bf16x8 yb[2];
#pragma unroll
      for (int j = 0; j < 2; ++j) yb[j] = *reinterpret_cast<const bf16x8*>(&sy[((2 * w + j) * 16 + fr) * PLD + k * 32 + fq * 8]);
#pragma unroll
      for (int i = 0; i < 7; ++i) {
        bf16x8 xa = *reinterpret_cast<const bf16x8*>(&sx[(i * 16 + fr) * PLD + ((((k * 4 + fq) ^ (i * 2 + (fr >> 3))) & 15) << 3)]);
#pragma unroll
        for (int j = 0; j < 2; ++j) acc[i][j] = mfma32(yb[j], xa, acc[i][j]);
      }
    }
  }
  u16* WT = (u16*)(p.ws + OFF_WT);
  const float sc = isU ? 1.f : 0.0625f * 1.4426950408889634f;
#pragma unroll
  for (int i = 0; i < 7; ++i)
#pragma unroll
    for (int j = 0; j < 2; ++j) {
      const int row = (isU ? HD : 0) + h * D_ + tr * 112 + i * 16 + fr;
      const int col = tc * 224 + (2 * w + j) * 16 + fq * 4;
      *reinterpret_cast<bf16x4*>(WT + (size_t)row * D_ + col) = cvt4(acc[i][j] * sc);
    }
}

constexpr int XS_LD = 456;
template <int NROWS>
__device__ __forceinline__ void stage_x_t(const Params& p, u16* xs, int row0) {
  const u16* xn = (const u16*)(p.ws + OFF_XN) + (size_t)row0 * D_;
  constexpr int N = NROWS * 56, IT = (N + 511) / 512;
  const int t = otid();
  bf16x8 v[IT];
#pragma unroll
  for (int u = 0; u < IT; ++u) { const int i = t + u * 512; if (i < N) v[u] = ldg8(xn + (size_t)i * 8); }
#pragma unroll
  for (int u = 0; u < IT; ++u) { const int i = t + u * 512; if (i < N) { const int r = i / 56, c = i % 56; *reinterpret_cast<bf16x8*>(&xs[r * XS_LD + c * 8]) = v[u]; } }
}

template <bool ISG>
__device__ __forceinline__ void producer_t(const Params& p, rsrc_t rs, char* smem, int nt) {
  const int tid = otid(), lane = tid & 63, w = tid >> 6, fr = lane & 15, fq = lane >> 4;
  u16* xs = (u16*)smem;
  u16* ot = xs + 2 * 32 * XS_LD;
  const u16* WT = (const u16*)(p.ws + OFF_WT);
  const u16* xn = (const u16*)(p.ws + OFF_XN);
  bf16x8 wb[14];
#pragma unroll
  for (int k = 0; k < 14; ++k) wb[k] = ldg8(WT + (size_t)(nt * 128 + w * 16 + fr) * D_ + k * 32 + fq * 8);
  bf16x8 xv[4];
#pragma unroll
  for (int u = 0; u < 4; ++u) { const int i = tid + u * 512; if (i < 1792) xv[u] = ldg8(xn + (size_t)i * 8); }
  for (int s = 0; s < NSEG; ++s) {
    u16* xb = xs + (s & 1) * 32 * XS_LD;
#pragma unroll
    for (int u = 0; u < 4; ++u) { const int i = tid + u * 512; if (i < 1792) { const int r = i / 56, c = i % 56; *reinterpret_cast<bf16x8*>(&xb[r * XS_LD + c * 8]) = xv[u]; } }
    asm volatile("s_waitcnt vmcnt(1)" ::: "memory");
    __syncthreads();
    if (s > 5 && tid == 0) __hip_atomic_fetch_add(cnt_ptr(p, CNT_GY + s - 2), 1u, __ATOMIC_RELAXED, __HIP_MEMORY_SCOPE_AGENT);
    if (s + 1 < NSEG) {
#pragma unroll
      for (int u = 0; u < 4; ++u) { const int i = tid + u * 512; if (i < 1792) xv[u] = ldg8(xn + (size_t)(s + 1) * 32 * D_ + (size_t)i * 8); }
    }
    f32x4 acc[2] = {};
#pragma unroll
    for (int k = 0; k < 14; ++k)
#pragma unroll
      for (int i = 0; i < 2; ++i) {
        bf16x8 xa = *reinterpret_cast<const bf16x8*>(&xb[(i * 16 + fr) * XS_LD + k * 32 + fq * 8]);
        acc[i] = ISG ? mfma32(wb[k], xa, acc[i]) : mfma32(xa, wb[k], acc[i]);
      }
    if (ISG) {
#pragma unroll
      for (int i = 0; i < 2; ++i) *reinterpret_cast<u32x2*>(&ot[(i * 16 + fr) * 136 + w * 16 + fq * 4]) = cvt4u(acc[i]);
    } else {
#pragma unroll
      for (int i = 0; i < 2; ++i) *reinterpret_cast<u32x2*>(&ot[(w * 16 + fr) * 36 + i * 16 + fq * 4]) = cvt4u(acc[i]);
    }
    __syncthreads();
    if (ISG) {
      const int r = tid >> 4, cch = tid & 15;
      const u32x2 a = *reinterpret_cast<const u32x2*>(&ot[r * 136 + cch * 8]), b2 = *reinterpret_cast<const u32x2*>(&ot[r * 136 + cch * 8 + 4]);
      u32x4 v = {a[0], a[1], b2[0], b2[1]};
      st128(rs, (unsigned)OFF_GT + (unsigned)((s * 32 + r) * HD + nt * 128 + cch * 8) * 2u, v);
    } else {
      const int row = tid >> 2, cch = tid & 3;
      const u32x2 a = *reinterpret_cast<const u32x2*>(&ot[row * 36 + cch * 8]), b2 = *reinterpret_cast<const u32x2*>(&ot[row * 36 + cch * 8 + 4]);
      u32x4 v = {a[0], a[1], b2[0], b2[1]};
      st128(rs, (unsigned)OFF_YT + (unsigned)((s * HD + nt * 128 - HD + row) * 32 + cch * 8) * 2u, v);
    }
    if (s < 4) signal_add(p, CNT_GY + s, 1u);
  }
  signal_add(p, CNT_GY + NSEG - 2, 1u);
  if (tid == 0) __hip_atomic_fetch_add(cnt_ptr(p, CNT_GY + NSEG - 1), 1u, __ATOMIC_RELAXED, __HIP_MEMORY_SCOPE_AGENT);
}

__device__ __forceinline__ void tr_item(const Params& p, rsrc_t rs, char* smem, int it) {
  const int tid = otid();
  u16* sx = (u16*)smem;
  for (int t = it * 8; t < it * 8 + 8; ++t) {
    const bool second = t >= 196; const int tt = second ? t - 196 : t;
    const u16* src = second ? p.w2 : p.w1;
    const unsigned dst = (unsigned)(second ? OFF_W2T : OFF_W1T);
    const int R = second ? F_ : D_, C = second ? D_ : F_;
    const int nct = C / 64, rt = tt / nct, ct = tt % nct;
    __syncthreads();
    { const int r = tid >> 3, c = tid & 7;
      bf16x8 v = ldg8(src + (size_t)(rt * 64 + r) * C + ct * 64 + c * 8);
#pragma unroll
      for (int j = 0; j < 8; ++j) sx[(c * 8 + j) * 72 + r] = (u16)v[j]; }
    __syncthreads();
    { const int c = tid >> 3, r = tid & 7;
      st128(rs, dst + (unsigned)((ct * 64 + c) * R + rt * 64 + r * 8) * 2u, *reinterpret_cast<const u32x4*>(&sx[c * 72 + r * 8])); }
  }
  signal_add(p, CNT_TR, 1u);
}

constexpr int HS_LD = 1800;
__device__ __forceinline__ float gelu_tanh(float x) {
  const float u = 0.7978845608028654f * (x + 0.044715f * x * x * x);
  const float e = __expf(2.f * u);
  const float th = 1.f - 2.f / (e + 1.f);
  return 0.5f * x * (1.f + th);
}
__device__ __forceinline__ void mlp_item(const Params& p, rsrc_t rs, char* smem, int rb) {
  const int tid = otid(), lane = tid & 63, w = tid >> 6, fr = lane & 15, fq = lane >> 4;
  u16* xs = (u16*)smem;
  u16* hs = xs + 32 * XS_LD;
  wait_cnt(p, CNT_TR, 49u);
  acquire_all();
  stage_x_t<32>(p, xs, rb * 32);
  __syncthreads();
  const u16* w1T = (const u16*)(p.ws + OFF_W1T);
  for (int nt = w; nt < 112; nt += 16) {
    f32x4 acc[2][2] = {};
    bf16x8 wb[2][14];
#pragma unroll
    for (int u = 0; u < 2; ++u)
#pragma unroll
      for (int k = 0; k < 14; ++k) wb[u][k] = ldg8(w1T + (size_t)((nt + 8 * u) * 16 + fr) * D_ + k * 32 + fq * 8);
#pragma unroll
    for (int k = 0; k < 14; ++k)
#pragma unroll
      for (int i = 0; i < 2; ++i) {
        bf16x8 xa = *reinterpret_cast<const bf16x8*>(&xs[(i * 16 + fr) * XS_LD + k * 32 + fq * 8]);
#pragma unroll
        for (int u = 0; u < 2; ++u) acc[u][i] = mfma32(wb[u][k], xa, acc[u][i]);
      }
#pragma unroll
    for (int u = 0; u < 2; ++u) {
      const int n = (nt + 8 * u) * 16 + fq * 4;
      bf16x4 bv = *reinterpret_cast<const bf16x4*>(p.b1 + n);
#pragma unroll
      for (int i = 0; i < 2; ++i) {
        f32x4 g;
#pragma unroll
        for (int r = 0; r < 4; ++r) g[r] = gelu_tanh(rbf(rbf(acc[u][i][r]) + bf2f((u16)bv[r])));
        *reinterpret_cast<bf16x4*>(&hs[(i * 16 + fr) * HS_LD + n]) = cvt4(g);
      }
    }
  }
  __syncthreads();
  const u16* w2T = (const u16*)(p.ws + OFF_W2T);
  const int kh = w & 1, ng = w >> 1;
  f32x4 acc[2][7] = {};
  for (int k4 = 0; k4 < 7; ++k4) {
    bf16x8 wb[4][7];
#pragma unroll
    for (int u = 0; u < 4; ++u)
#pragma unroll
      for (int j = 0; j < 7; ++j) wb[u][j] = ldg8(w2T + (size_t)((ng * 7 + j) * 16 + fr) * F_ + kh * 896 + (k4 * 4 + u) * 32 + fq * 8);
#pragma unroll
    for (int u = 0; u < 4; ++u) {
      const int kk = kh * 896 + (k4 * 4 + u) * 32 + fq * 8;
#pragma unroll
      for (int i = 0; i < 2; ++i) {
        bf16x8 ha = *reinterpret_cast<const bf16x8*>(&hs[(i * 16 + fr) * HS_LD + kk]);
#pragma unroll
        for (int j = 0; j < 7; ++j) acc[i][j] = mfma32(wb[u][j], ha, acc[i][j]);
      }
    }
  }
  __syncthreads();
  float* red = (float*)smem;
  if (kh == 1) {
#pragma unroll
    for (int i = 0; i < 2; ++i)
#pragma unroll
      for (int j = 0; j < 7; ++j) *reinterpret_cast<f32x4*>(&red[(i * 16 + fr) * D_ + (ng * 7 + j) * 16 + fq * 4]) = acc[i][j];
  }
  __syncthreads();
  if (kh == 0) {
#pragma unroll
    for (int i = 0; i < 2; ++i)
#pragma unroll
      for (int j = 0; j < 7; ++j) {
        const int n = (ng * 7 + j) * 16 + fq * 4;
        f32x4 o = acc[i][j] + *reinterpret_cast<const f32x4*>(&red[(i * 16 + fr) * D_ + n]);
        bf16x4 bv = *reinterpret_cast<const bf16x4*>(p.b2 + n);
#pragma unroll
        for (int r = 0; r < 4; ++r) o[r] = rbf(o[r]) + bf2f((u16)bv[r]);
        st64(rs, (unsigned)OFF_MLP + (unsigned)((rb * 32 + i * 16 + fr) * D_ + n) * 2u, cvt4u(o));
      }
  }
  signal_add(p, CNT_MLP + rb, 1u);
}

__device__ __forceinline__ void sss_item(const Params& p, rsrc_t rs, int s) {
  const int tid = otid(), lane = tid & 63, w = tid >> 6, fr = lane & 15, fq = lane >> 4;
  wait_cnt(p, CNT_GY + s, 112u);
  acquire_all();
  const u16* xn = (const u16*)(p.ws + OFF_XN) + (size_t)s * 32 * D_;
  const u16* Gt = (const u16*)(p.ws + OFF_GT) + (size_t)s * 32 * HD;
  for (int hh = 0; hh < 2; ++hh) {
    const int h = w * 2 + hh;
    f32x4 acc[2][2] = {};
#pragma unroll
    for (int k = 0; k < 14; ++k) {
      bf16x8 xa[2], gb[2];
#pragma unroll
      for (int i = 0; i < 2; ++i) {
        xa[i] = ldg8(xn + (size_t)(i * 16 + fr) * D_ + k * 32 + fq * 8);
        gb[i] = ldg8(Gt + (size_t)(i * 16 + fr) * HD + h * D_ + k * 32 + fq * 8);
      }
#pragma unroll
      for (int i = 0; i < 2; ++i)
#pragma unroll
        for (int j = 0; j < 2; ++j) acc[i][j] = mfma32(gb[j], xa[i], acc[i][j]);
    }
#pragma unroll
    for (int i = 0; i < 2; ++i)
#pragma unroll
      for (int j = 0; j < 2; ++j)
        st128(rs, (unsigned)OFF_SSS + (unsigned)(((s * H_ + h) * 32 + i * 16 + fr) * 32 + j * 16 + fq * 4) * 4u, __builtin_bit_cast(u32x4, acc[i][j]));
  }
  signal_add(p, CNT_SSS + s, 1u);
}

constexpr int PS_LD = 648;
__device__ __forceinline__ void tail_item(const Params& p, rsrc_t rs, char* smem, int s, int cq) {
  const int tid = otid(), lane = tid & 63, w = tid >> 6, fr = lane & 15, fq = lane >> 4;
  float* ssm = (float*)smem;
  u16* ps = (u16*)(smem + 16 * 32 * 8 * 4);
  u16* xs = ps + 32 * PS_LD;
  wait_cnt(p, CNT_SSS + s, 1u);
  wait_cnt(p, CNT_MLP + s, 1u);
  acquire_all();
  stage_x_t<32>(p, xs, s * 32);
  const int hrow = tid >> 5, trow = tid & 31;
  f32x4 sv[8];
  {
    const float* sr = (const float*)(p.ws + OFF_SSS) + (((size_t)s * H_ + hrow) * 32 + trow) * 32;
#pragma unroll
    for (int k = 0; k < 8; ++k) sv[k] = *reinterpret_cast<const f32x4*>(sr + k * 4);
  }
  bf16x8 wb[20];
  const int dcol = cq * 112 + (w < 7 ? w : 0) * 16 + fr;
  {
    const u16* Yt = (const u16*)(p.ws + OFF_YT) + (size_t)s * HD * 32;
#pragma unroll
    for (int k = 0; k < 16; ++k) wb[k] = ldg8(Yt + ((size_t)k * D_ + dcol) * 32 + fq * 8);
  }
  f32x4 base[2];
  {
    const u16* mo = (const u16*)(p.ws + OFF_MLP);
#pragma unroll
    for (int i = 0; i < 2; ++i) {
      const size_t o = (size_t)(s * 32 + i * 16 + fr) * D_ + cq * 112 + (w < 7 ? w : 0) * 16 + fq * 4;
      u32x2 xv = *reinterpret_cast<const u32x2*>(p.x + o), bv = *reinterpret_cast<const u32x2*>(mo + o);
      base[i][0] = __uint_as_float(xv[0]); base[i][1] = __uint_as_float(xv[1]); base[i][2] = __uint_as_float(bv[0]); base[i][3] = __uint_as_float(bv[1]);
    }
  }
  if (tid == 0) {
    unsigned spins = 0;
    if (s < NSEG - 1) {
      while (__hip_atomic_load(cnt_ptr(p, CNT_REC + s), __ATOMIC_RELAXED, __HIP_MEMORY_SCOPE_AGENT) < 64u) { __builtin_amdgcn_s_sleep(127); if (++spins > SPIN_CAP) break; }
    } else {
      while (__hip_atomic_load(cnt_ptr(p, CNT_REC + s - 1), __ATOMIC_RELAXED, __HIP_MEMORY_SCOPE_AGENT) < 64u) { __builtin_amdgcn_s_sleep(127); if (++spins > SPIN_CAP) break; }
      spins = 0;
      while (__hip_atomic_load(cnt_ptr(p, CNT_REC + s), __ATOMIC_RELAXED, __HIP_MEMORY_SCOPE_AGENT) < 64u) { __builtin_amdgcn_s_sleep(2); if (++spins > SPIN_CAP) break; }
    }
  }
  __syncthreads();
  {
    bf16x8 gb[14];
#pragma unroll
    for (int k = 0; k < 14; ++k) {
      const int d = k * 32 + fq * 8, hh2 = w * 2 + (fr >> 3), jq2 = d / 112, dd = d - jq2 * 112;
      gb[k] = ld8s(rs, (unsigned)OFF_GM + (unsigned)(s * (H_ * M_ * D_) + (((hh2 * 4 + jq2) * 7 + (dd >> 4)) * 8 + (fr & 7)) * 16 + (dd & 15)) * 2u);
    }
#pragma unroll
    for (int k = 16; k < 20; ++k) {
      const int hz = (k - 16) * 4 + fq;
      wb[k] = ld8s(rs, (unsigned)OFF_ZM + (unsigned)(s * (H_ * D_ * M_) + (((dcol >> 3) * 8 + (hz >> 1)) * 16 + (hz & 1) * 8 + (dcol & 7)) * 8) * 2u);
    }
    f32x4 acc[2] = {};
#pragma unroll
    for (int k = 0; k < 14; ++k)
#pragma unroll
      for (int i = 0; i < 2; ++i) {
        bf16x8 xa = *reinterpret_cast<const bf16x8*>(&xs[(i * 16 + fr) * XS_LD + k * 32 + fq * 8]);
        acc[i] = mfma32(gb[k], xa, acc[i]);
      }
#pragma unroll
    for (int i = 0; i < 2; ++i)
      *reinterpret_cast<f32x4*>(&ssm[((w * 2 + (fq >> 1)) * 32 + i * 16 + fr) * 8 + (fq & 1) * 4]) = acc[i];
  }
  __syncthreads();
  {
    const int h = hrow, t = trow;
    float a[32], m[8];
#pragma unroll
    for (int k = 0; k < 8; ++k) { a[4 * k] = sv[k][0]; a[4 * k + 1] = sv[k][1]; a[4 * k + 2] = sv[k][2]; a[4 * k + 3] = sv[k][3]; }
#pragma unroll
    for (int j = 0; j < 8; ++j) m[j] = ssm[(h * 32 + t) * 8 + j];
    float mx = m[0];
#pragma unroll
    for (int j = 1; j < 8; ++j) mx = fmaxf(mx, m[j]);
#pragma unroll
    for (int k = 0; k < 32; ++k) if (k <= t) mx = fmaxf(mx, a[k]);
    float sum = 0.f;
#pragma unroll
    for (int j = 0; j < 8; ++j) { m[j] = ex2(m[j] - mx); sum += m[j]; }
#pragma unroll
    for (int k = 0; k < 32; ++k) { a[k] = (k <= t) ? ex2(a[k] - mx) : 0.f; sum += a[k]; }
    const float inv = 1.f / sum;
#pragma unroll
    for (int k = 0; k < 4; ++k) {
      u32x4 v;
#pragma unroll
      for (int r = 0; r < 4; ++r) v[r] = pk2(a[8 * k + 2 * r] * inv, a[8 * k + 2 * r + 1] * inv);
      *reinterpret_cast<u32x4*>(&ps[t * PS_LD + h * 32 + k * 8]) = v;
    }
    u32x4 v;
#pragma unroll
    for (int r = 0; r < 4; ++r) v[r] = pk2(m[2 * r] * inv, m[2 * r + 1] * inv);
    *reinterpret_cast<u32x4*>(&ps[t * PS_LD + 512 + h * 8]) = v;
  }
  __syncthreads();
  if (w < 7) {
    f32x4 acc[2] = {};
#pragma unroll
    for (int k = 0; k < 20; ++k) {
#pragma unroll
      for (int i = 0; i < 2; ++i) {
        bf16x8 pa = *reinterpret_cast<const bf16x8*>(&ps[(i * 16 + fr) * PS_LD + k * 32 + fq * 8]);
        acc[i] = mfma32(wb[k], pa, acc[i]);
      }
    }
#pragma unroll
    for (int i = 0; i < 2; ++i) {
      const size_t o = (size_t)(s * 32 + i * 16 + fr) * D_ + cq * 112 + w * 16 + fq * 4;
      const unsigned x0 = __float_as_uint(base[i][0]), x1 = __float_as_uint(base[i][1]), b0 = __float_as_uint(base[i][2]), b1 = __float_as_uint(base[i][3]);
      f32x4 r;
      r[0] = rbf(lo16(x0) + rbf(acc[i][0])) + lo16(b0);
      r[1] = rbf(hi16(x0) + rbf(acc[i][1])) + hi16(b0);
      r[2] = rbf(lo16(x1) + rbf(acc[i][2])) + lo16(b1);
      r[3] = rbf(hi16(x1) + rbf(acc[i][3])) + hi16(b1);
      *reinterpret_cast<bf16x4*>(p.out + o) = cvt4(r);
    }
  }
  __syncthreads();
}

constexpr int IT_TR = 49, IT_MLP = 113, IT_SSS = 177, IT_END = 433;
__device__ __forceinline__ void worker(const Params& p, char* smem) {
  const rsrc_t rs = ws_rsrc(p);
  unsigned* sitem = (unsigned*)(smem + 152 * 1024 - 16);
  if (blockIdx.x < 64 + 112) {
    const int nt = blockIdx.x - 64;
    if (nt < 56) producer_t<true>(p, rs, smem, nt); else producer_t<false>(p, rs, smem, nt);
  }
  for (;;) {
    __syncthreads();
    if (threadIdx.x == 0) *sitem = __hip_atomic_fetch_add(cnt_ptr(p, CNT_Q), 1u, __ATOMIC_RELAXED, __HIP_MEMORY_SCOPE_AGENT);
    __syncthreads();
    const int it = (int)*sitem;
    if (it >= IT_END) break;
    if (it < IT_TR) tr_item(p, rs, smem, it);
    else if (it < IT_MLP) mlp_item(p, rs, smem, it - IT_TR);
    else if (it < IT_SSS) sss_item(p, rs, it - IT_MLP);
    else { const int q = it - IT_SSS; tail_item(p, rs, smem, q >> 2, q & 3); }
  }
}

__device__ __forceinline__ void recurrence(const Params& p, char* smem) {
  u16* ms = (u16*)smem;
  float* red1 = (float*)(smem + 7168);
  float* red2 = red1 + 7 * 64;
  u16* zs = (u16*)(red2 + 16 * 64);
  u32x4* gbuf = (u32x4*)(smem + 16384);
  u32x4* ybuf = (u32x4*)(smem + 16384 + 28672);
  const unsigned lds0 = __builtin_amdgcn_readfirstlane((unsigned)(size_t)(LAS char*)smem);
  const int tid = threadIdx.x, lane = tid & 63, w = tid >> 6, fr = lane & 15, fq = lane >> 4;
  const int c = blockIdx.x, h = c >> 2, jq = c & 3;
  const bool isval = c < 56;
  const u16* WT = (const u16*)(p.ws + OFF_WT);
  const rsrc_t rs = ws_rsrc(p);
  const unsigned g_off = (unsigned)OFF_GT + (unsigned)((jq * 8 + (fr & 7)) * HD + h * D_ + fq * 8) * 2u;
  const int zh = 2 * w + (fr >> 3), zd = (isval ? 8 * c : 0) + (fr & 7);

  bf16x8 wa[14], wz[14];
  if (w < 7) {
#pragma unroll
    for (int k = 0; k < 14; ++k) wa[k] = ldg8(WT + (size_t)(h * D_ + jq * 112 + w * 16 + fr) * D_ + k * 32 + fq * 8);
  }
#pragma unroll
  for (int k = 0; k < 14; ++k) wz[k] = ldg8(WT + (size_t)(HD + zh * D_ + zd) * D_ + k * 32 + fq * 8);
  auto prefetch = [&](int sg) {
    const char* gp = p.ws + g_off + (size_t)sg * (32 * HD * 2);
#pragma unroll
    for (int k = 0; k < 14; ++k) glds16_sc1(gp + k * 64, lds0 + 16384 + (sg & 1) * 14336 + k * 1024);
    if (isval) {
      const char* yp = p.ws + OFF_YT + (size_t)(((fr >> 3) * D_ + zd) * 32 + fq * 8) * 2 + (size_t)sg * (HD * 32 * 2);
#pragma unroll
      for (int k = 0; k < 8; ++k) glds16_sc1(yp + (size_t)k * (2 * D_ * 32 * 2), lds0 + 16384 + 28672 + (sg & 1) * 8192 + k * 1024);
    }
  };
  if (w == 7) {
    unsigned spins = 0;
    while (__hip_atomic_load(cnt_ptr(p, CNT_GY), __ATOMIC_RELAXED, __HIP_MEMORY_SCOPE_AGENT) < 112u) { __builtin_amdgcn_s_sleep(2); if (++spins > SPIN_CAP) break; }
    prefetch(0);
  }
  __builtin_amdgcn_s_waitcnt(0);
  for (int s = 0; s < NSEG; ++s) {
    if (w < 7) {
      const int id = w * 64 + lane, cc = id >> 3, i = id & 7;
      const unsigned off = (unsigned)OFF_MEMB + s * (M_ * D_ * 2) + id * 16;
      u32x4 v; unsigned spins = 0;
      if (s > 0) { if (!isval) __builtin_amdgcn_s_sleep(SLN); __builtin_amdgcn_s_sleep(SLX); }
      for (;;) {
        asm volatile("" ::: "memory");
        v = ld128(rs, off);
        if (!__any(has_sent(v))) break;
        if (++spins > SPIN_CAP) break;
      }
      *reinterpret_cast<u32x4*>(&ms[((cc >> 3) * 64 + (cc & 3) * 16 + ((cc >> 2) & 1) * 8 + i) * 8]) = v;
    } else {
      asm volatile("s_waitcnt vmcnt(0)" ::: "memory");
      if (s <= 1) {
        unsigned spins = 0;
        while (__hip_atomic_load(cnt_ptr(p, CNT_GY + s + 1), __ATOMIC_RELAXED, __HIP_MEMORY_SCOPE_AGENT) < 112u) { __builtin_amdgcn_s_sleep(2); if (++spins > SPIN_CAP) break; }
      }
      if (s + 1 < NSEG) prefetch(s + 1);
#pragma unroll
      for (int k = 0; k < 14; ++k) wa[k] = __builtin_bit_cast(bf16x8, gbuf[((s & 1) * 14 + k) * 64 + lane]);
    }
    __syncthreads();
    bf16x8 mf[14];
#pragma unroll
    for (int k = 0; k < 7; ++k) mf[2 * k] = *reinterpret_cast<const bf16x8*>(&ms[(k * 64 + lane) * 8]);
    bf16x4 m4;
    { const int d0 = jq * 112 + (w < 7 ? w : 0) * 16 + fq * 4, cc = d0 >> 3;
      m4 = *reinterpret_cast<const bf16x4*>(&ms[((cc >> 3) * 64 + (cc & 3) * 16 + ((cc >> 2) & 1) * 8 + (fr & 7)) * 8 + (d0 & 4)]); }
    __builtin_amdgcn_sched_barrier(0);
#pragma unroll
    for (int k = 0; k < 7; ++k) {
      u32x4 t = __builtin_bit_cast(u32x4, mf[2 * k]), r;
#pragma unroll
      for (int e = 0; e < 4; ++e) r[e] = __builtin_amdgcn_mov_dpp(t[e], 0x128  , 0xF, 0xF, false);
      mf[2 * k + 1] = __builtin_bit_cast(bf16x8, r);
    }
    f32x4 acc = {}, acc1 = {};
#pragma unroll
    for (int k = 0; k < 7; ++k) { acc = mfma32(wa[2 * k], mf[2 * k], acc); acc1 = mfma32(wa[2 * k + 1], mf[2 * k + 1], acc1); }
    acc += acc1;
    u32x2 gbu = cvt4u(acc);
    bf16x4 gb = __builtin_bit_cast(bf16x4, gbu);
    if (w < 7) {
      f32x4 z4 = {};
      f32x4 sp = mfma16(gb, m4, z4);
      if (fr < 8 && fq < 2) *reinterpret_cast<f32x4*>(&red1[w * 64 + fr * 8 + fq * 4]) = sp;
      if (fr < 8) st64(rs, (unsigned)OFF_GM + (unsigned)((((s * 64 + c) * 7 + w) * 8 + fr) * 16 + fq * 4) * 2u, gbu);
    } else {
      if (fr < 8 && fq < 2) {
        u32x2 o; o[0] = canon(gbu[0]); o[1] = canon(gbu[1]);
        st64(rs, (unsigned)OFF_SB + s * SREC + c * 256 + fr * 16 + fq * 8, o);
      }
    }
    __syncthreads();
    if (w == 0 && lane < 32) {
      f32x2 t = *reinterpret_cast<const f32x2*>(&red1[lane * 2]);
#pragma unroll
      for (int k = 1; k < 7; ++k) t += *reinterpret_cast<const f32x2*>(&red1[k * 64 + lane * 2]);
      st32(rs, (unsigned)OFF_SB + s * SREC + c * 256 + 128 + lane * 4, canon(pk2(t[0], t[1])));
    }
    if (s > 0 && tid == 64) __hip_atomic_fetch_add(cnt_ptr(p, CNT_REC + s - 1), 1u, __ATOMIC_RELAXED, __HIP_MEMORY_SCOPE_AGENT);
    const bool need_gy = (s + 2) < NSEG && s >= 1;
    if (isval) {
      f32x4 az = {};
#pragma unroll
      for (int k = 0; k < 14; ++k) az = mfma32(mf[k], wz[k], az);
      u32x2 zbu = cvt4u(az);
      if (fq < 2) {
        st64(rs, (unsigned)OFF_ZM + (unsigned)((((s * 56 + c) * 8 + w) * 16 + fr) * 8 + fq * 4) * 2u, zbu);
        *reinterpret_cast<u32x2*>(&zs[(w * 16 + fr) * 8 + fq * 4]) = zbu;
      }
      const u32x4 yfu = ybuf[((s & 1) * 8 + w) * 64 + lane];
      u32x4 zf = {0u, 0u, 0u, 0u};
      zf[0] = *reinterpret_cast<const unsigned*>(&zs[(w * 16 + fr) * 8 + fq * 2]);
      const int hh = fr >> 3, i = fr & 7, hd = 2 * w + hh;
      const unsigned o1 = (unsigned)OFF_SB + s * SREC + (hd * 4 + fq) * 256 + i * 16;
      const unsigned o2 = (unsigned)OFF_SB + s * SREC + hd * 1024 + 128 + i * 16 + fq * 4;
      const unsigned o3 = (unsigned)OFF_CNT + (unsigned)(CNT_GY + (need_gy ? (s + 2) : 0)) * 64u;
      u32x4 a0; unsigned b0, b1, b2, b3, gc; unsigned spins = 0;
      __builtin_amdgcn_s_sleep(SLY);
      for (;;) {
        asm volatile("" ::: "memory");
        a0 = ld128(rs, o1);
        b0 = ld32(rs, o2); b1 = ld32(rs, o2 + 256); b2 = ld32(rs, o2 + 512); b3 = ld32(rs, o2 + 768);
        gc = ld32(rs, o3);
        if (!__any(has_sent(a0) || b0 == SENT || b1 == SENT || b2 == SENT || b3 == SENT || gc < 112u)) break;
        if (++spins > SPIN_CAP) break;
      }
      float sg[8];
#pragma unroll
      for (int r = 0; r < 4; ++r) { sg[2 * r] = lo16(a0[r]); sg[2 * r + 1] = hi16(a0[r]); }
      float m0 = (lo16(b0) + lo16(b1)) + (lo16(b2) + lo16(b3));
      float m1 = (hi16(b0) + hi16(b1)) + (hi16(b2) + hi16(b3));
      float mx = fmaxf(fmaxf(fmaxf(sg[0], sg[1]), fmaxf(sg[2], sg[3])), fmaxf(fmaxf(sg[4], sg[5]), fmaxf(sg[6], sg[7])));
      mx = fmaxf(mx, fmaxf(m0, m1));
      mx = xmax32(xmax16(mx));
      m0 = ex2(m0 - mx); m1 = ex2(m1 - mx);
      float ss = m0 + m1;
#pragma unroll
      for (int r = 0; r < 8; ++r) { sg[r] = ex2(sg[r] - mx); ss += sg[r]; }
      ss = xsum32(xsum16(ss));
      const float inv = __builtin_amdgcn_rcpf(ss);
      u32x4 pa, pm = {0u, 0u, 0u, 0u};
#pragma unroll
      for (int r = 0; r < 4; ++r) pa[r] = pk2(sg[2 * r] * inv, sg[2 * r + 1] * inv);
      pm[0] = pk2(m0 * inv, m1 * inv);
      f32x4 o = {};
      o = mfma32(__builtin_bit_cast(bf16x8, yfu), __builtin_bit_cast(bf16x8, pa), o);
      o = mfma32(__builtin_bit_cast(bf16x8, zf), __builtin_bit_cast(bf16x8, pm), o);
      if ((fr >> 3) == (fq >> 1)) *reinterpret_cast<f32x4*>(&red2[(w * 2 + (fq >> 1)) * 64 + (fr & 7) * 8 + (fq & 1) * 4]) = o;
      __syncthreads();
      if (w == 0) {
        const int hb = (lane >> 5) * 8, l2 = (lane & 31) * 2;
        f32x2 t = *reinterpret_cast<const f32x2*>(&red2[hb * 64 + l2]);
#pragma unroll
        for (int k = 1; k < 8; ++k) t += *reinterpret_cast<const f32x2*>(&red2[(hb + k) * 64 + l2]);
        const float t0 = xsum32(t[0]), t1 = xsum32(t[1]);
        if (lane < 32) st32(rs, (unsigned)OFF_MEMB + (s + 1) * (M_ * D_ * 2) + c * 128 + lane * 4, canon(pk2(t0, t1)));
      }
    } else if (need_gy) {
      unsigned spins = 0;
      while (__hip_atomic_load(cnt_ptr(p, CNT_GY + s + 2), __ATOMIC_RELAXED, __HIP_MEMORY_SCOPE_AGENT) < 112u) { __builtin_amdgcn_s_sleep(2); if (++spins > SPIN_CAP) break; }
    }
  }
  signal_add(p, CNT_REC + NSEG - 1, 1u);
}

constexpr int LDS_MAIN = 152 * 1024;
__global__ __launch_bounds__(512, 2) void main_kernel(Params p) {
  extern __shared__ __attribute__((aligned(16))) char smem[];
  if (blockIdx.x < 64) recurrence(p, smem);
  else worker(p, smem);
}

extern "C" void kernel_launch(void* const* d_in, const int* in_sizes, int n_in, void* d_out, int out_size, void* d_ws,
                              size_t ws_size, hipStream_t stream) {
  Params p;
  p.x = (const u16*)d_in[0]; p.mem0 = (const u16*)d_in[1]; p.Wq = (const u16*)d_in[2]; p.Wk = (const u16*)d_in[3];
  p.Wv = (const u16*)d_in[4]; p.Wo = (const u16*)d_in[5]; p.w1 = (const u16*)d_in[8]; p.b1 = (const u16*)d_in[9];
  p.w2 = (const u16*)d_in[10]; p.b2 = (const u16*)d_in[11];
  p.out = (u16*)d_out; p.ws = (char*)d_ws;
  (void)hipFuncSetAttribute((const void*)main_kernel, hipFuncAttributeMaxDynamicSharedMemorySize, LDS_MAIN);
  prep_kernel<<<256, 448, 0, stream>>>(p);
  main_kernel<<<256, 512, LDS_MAIN, stream>>>(p);
}
```

```cpp
#include <hip/hip_runtime.h>
#include <hip/hip_bf16.h>
#include <stdint.h>

typedef __attribute__((ext_vector_type(8))) short bf16x8;
typedef __attribute__((ext_vector_type(4))) short bf16x4;
typedef __attribute__((ext_vector_type(4))) float f32x4;
typedef __attribute__((ext_vector_type(2))) float f32x2;
typedef __attribute__((ext_vector_type(4))) unsigned u32x4;
typedef __attribute__((ext_vector_type(2))) unsigned u32x2;
typedef __attribute__((ext_vector_type(2))) __bf16 bf2_t;
typedef unsigned short u16;
typedef __attribute__((address_space(1))) unsigned gu32;

constexpr int S_ = 2048, D_ = 448, H_ = 16, E_ = 256, M_ = 8, NSEG = 64, F_ = 1792;
constexpr int HD = H_ * D_;
constexpr int NW = 2 * HD;
constexpr int AUX_SC1 = 16;
constexpr unsigned SENT = 0xFFFFFFFFu;

constexpr size_t al(size_t x) { return (x + 255) & ~(size_t)255; }
constexpr size_t OFF_MEMB = 0;
constexpr size_t SZ_MEMB = (size_t)(NSEG + 1) * M_ * D_ * 2;
constexpr int SREC = 16384;
constexpr size_t OFF_SB = al(OFF_MEMB + SZ_MEMB);
constexpr size_t SZ_SB = (size_t)NSEG * SREC;
constexpr size_t SZ_HAND = al(OFF_SB + SZ_SB);
constexpr size_t OFF_CNT = SZ_HAND;
constexpr int CNT_Q = 0, CNT_TR = 1, CNT_GY = 2, CNT_MLP = 66, CNT_SSS = 130, CNT_REC = 194, CNT_TMO = 258, CNT_N = 259;
constexpr size_t OFF_XN = al(OFF_CNT + CNT_N * 64);
constexpr size_t OFF_WT = al(OFF_XN + (size_t)S_ * D_ * 2);
constexpr size_t OFF_GT = al(OFF_WT + (size_t)NW * D_ * 2);
constexpr size_t OFF_YT = al(OFF_GT + (size_t)S_ * HD * 2);
constexpr size_t OFF_W1T = al(OFF_YT + (size_t)S_ * HD * 2);
constexpr size_t OFF_W2T = al(OFF_W1T + (size_t)F_ * D_ * 2);
constexpr size_t OFF_MLP = al(OFF_W2T + (size_t)F_ * D_ * 2);
constexpr size_t OFF_GM = al(OFF_MLP + (size_t)S_ * D_ * 2);
constexpr size_t OFF_ZM = al(OFF_GM + (size_t)NSEG * H_ * M_ * D_ * 2);
constexpr size_t OFF_SSS = al(OFF_ZM + (size_t)NSEG * H_ * M_ * D_ * 2);
constexpr size_t WS_END = al(OFF_SSS + (size_t)NSEG * H_ * 32 * 32 * 4);

__device__ __forceinline__ float bf2f(u16 v) { return __uint_as_float(((unsigned)v) << 16); }
__device__ __forceinline__ u16 f2bf(float f) { __bf16 b = (__bf16)f; return __builtin_bit_cast(u16, b); }
__device__ __forceinline__ float rbf(float f) { return bf2f(f2bf(f)); }
__device__ __forceinline__ unsigned pk2(float a, float b) { f32x2 v = {a, b}; bf2_t r = __builtin_convertvector(v, bf2_t); return __builtin_bit_cast(unsigned, r); }
__device__ __forceinline__ float lo16(unsigned u) { return __uint_as_float(u << 16); }
__device__ __forceinline__ float hi16(unsigned u) { return __uint_as_float(u & 0xFFFF0000u); }
__device__ __forceinline__ float ex2(float x) { return __builtin_amdgcn_exp2f(x); }
__device__ __forceinline__ f32x4 mfma32(bf16x8 a, bf16x8 b, f32x4 c) { return __builtin_amdgcn_mfma_f32_16x16x32_bf16(a, b, c, 0, 0, 0); }
__device__ __forceinline__ f32x4 mfma16(bf16x4 a, bf16x4 b, f32x4 c) { return __builtin_amdgcn_mfma_f32_16x16x16bf16_1k(a, b, c, 0, 0, 0); }
__device__ __forceinline__ bf16x8 ldg8(const u16* p) { return *reinterpret_cast<const bf16x8*>(p); }
__device__ __forceinline__ u32x2 cvt4u(f32x4 v) { u32x2 r; r[0] = pk2(v[0], v[1]); r[1] = pk2(v[2], v[3]); return r; }
__device__ __forceinline__ bf16x4 cvt4(f32x4 v) { return __builtin_bit_cast(bf16x4, cvt4u(v)); }
__device__ __forceinline__ float xsum16(float x) { unsigned u = __float_as_uint(x); u32x2 r = __builtin_amdgcn_permlane16_swap(u, u, false, false); return __uint_as_float(r[0]) + __uint_as_float(r[1]); }
__device__ __forceinline__ float xsum32(float x) { unsigned u = __float_as_uint(x); u32x2 r = __builtin_amdgcn_permlane32_swap(u, u, false, false); return __uint_as_float(r[0]) + __uint_as_float(r[1]); }
__device__ __forceinline__ float xmax16(float x) { unsigned u = __float_as_uint(x); u32x2 r = __builtin_amdgcn_permlane16_swap(u, u, false, false); return fmaxf(__uint_as_float(r[0]), __uint_as_float(r[1])); }
__device__ __forceinline__ float xmax32(float x) { unsigned u = __float_as_uint(x); u32x2 r = __builtin_amdgcn_permlane32_swap(u, u, false, false); return fmaxf(__uint_as_float(r[0]), __uint_as_float(r[1])); }
__device__ __forceinline__ bool has_sent(u32x4 v) { return v[0] == SENT || v[1] == SENT || v[2] == SENT || v[3] == SENT; }
__device__ __forceinline__ unsigned canon(unsigned v) { return v == SENT ? 0x7FC07FC0u : v; }

__device__ __forceinline__ int otid() { int t = threadIdx.x; asm volatile("" : "+v"(t)); return t; }
#define LAS __attribute__((address_space(3)))
__device__ __forceinline__ void glds16_sc1(const void* gsrc, unsigned lds_dst) {
  unsigned keep;
  asm volatile("s_mov_b32 %0, m0\n\ts_mov_b32 m0, %2\n\ts_nop 0\n\tglobal_load_lds_dwordx4 %1, off sc1\n\ts_mov_b32 m0, %0" : "=&s"(keep) : "v"(gsrc), "s"(lds_dst) : "memory");
}
struct Params {
  const u16 *x, *mem0, *Wq, *Wk, *Wv, *Wo, *w1, *b1, *w2, *b2;
  u16* out;
  char* ws;
};
typedef __amdgpu_buffer_rsrc_t rsrc_t;
__device__ __forceinline__ rsrc_t ws_rsrc(const Params& p) { return __builtin_amdgcn_make_buffer_rsrc(p.ws, 0, (int)WS_END, 0x00020000); }
__device__ __forceinline__ void st64(rsrc_t rs, unsigned off, u32x2 v) { __builtin_amdgcn_raw_buffer_store_b64(v, rs, (int)off, 0, AUX_SC1); }
__device__ __forceinline__ void st128(rsrc_t rs, unsigned off, u32x4 v) { __builtin_amdgcn_raw_buffer_store_b128(v, rs, (int)off, 0, AUX_SC1); }
__device__ __forceinline__ void st32(rsrc_t rs, unsigned off, unsigned v) { __builtin_amdgcn_raw_buffer_store_b32(v, rs, (int)off, 0, AUX_SC1); }
__device__ __forceinline__ u32x4 ld128(rsrc_t rs, unsigned off) { return __builtin_amdgcn_raw_buffer_load_b128(rs, (int)off, 0, AUX_SC1); }
__device__ __forceinline__ unsigned ld32(rsrc_t rs, unsigned off) { return __builtin_amdgcn_raw_buffer_load_b32(rs, (int)off, 0, AUX_SC1); }
__device__ __forceinline__ bf16x8 ld8s(rsrc_t rs, unsigned off) { return __builtin_bit_cast(bf16x8, ld128(rs, off)); }
__device__ __forceinline__ gu32* cnt_ptr(const Params& p, int i) { return (gu32*)(p.ws + OFF_CNT + (size_t)i * 64); }
__device__ __forceinline__ bool spin_fail(const Params& p, unsigned& spins, unsigned cap) {
  if (++spins > cap) { __hip_atomic_store(cnt_ptr(p, CNT_TMO), 1u, __ATOMIC_RELAXED, __HIP_MEMORY_SCOPE_AGENT); return true; }
  return (spins & 1023u) == 0u && __hip_atomic_load(cnt_ptr(p, CNT_TMO), __ATOMIC_RELAXED, __HIP_MEMORY_SCOPE_AGENT) != 0u;
}
constexpr unsigned CAP_FAST = 1u << 20, CAP_SLOW = 1u << 18;
constexpr int SLX = 22;
constexpr int SLN = 36;
constexpr int SLY = 6;

__device__ __forceinline__ void signal_add(const Params& p, int cnt, unsigned v) {
  asm volatile("s_waitcnt vmcnt(0)" ::: "memory");
  __syncthreads();
  if (threadIdx.x == 0) __hip_atomic_fetch_add(cnt_ptr(p, cnt), v, __ATOMIC_RELAXED, __HIP_MEMORY_SCOPE_AGENT);
}
__device__ __forceinline__ void wait_cnt(const Params& p, int cnt, unsigned want) {
  if (threadIdx.x == 0) {
    unsigned spins = 0;
    while (__hip_atomic_load(cnt_ptr(p, cnt), __ATOMIC_RELAXED, __HIP_MEMORY_SCOPE_AGENT) < want) {
      __builtin_amdgcn_s_sleep(64);
      if (spin_fail(p, spins, CAP_SLOW)) break;
    }
  }
}
__device__ __forceinline__ void acquire_all() {
  if (threadIdx.x == 0) { __builtin_amdgcn_fence(__ATOMIC_ACQUIRE, "agent"); asm volatile("s_waitcnt vmcnt(0)" ::: "memory"); }
  __syncthreads();
}

constexpr int PLD = 136;
__global__ __launch_bounds__(448) void prep_kernel(Params p) {
  __shared__ __attribute__((aligned(16))) u16 sx[112 * PLD];
  __shared__ __attribute__((aligned(16))) u16 sy[224 * PLD];
  const int tid = threadIdx.x, lane = tid & 63, w = tid >> 6, fr = lane & 15, fq = lane >> 4;
  const int b = blockIdx.x;
  const int mat = (b & 7) * 4 + (b >> 6), tr = (b >> 4) & 3, tc = (b >> 3) & 1;
  const int h = mat & 15; const bool isU = mat >= 16;
  const u16* Yop = isU ? p.Wv : p.Wk;
  bf16x8 vx[2][4], vy[2][8];
#pragma unroll
  for (int kh = 0; kh < 2; ++kh) {
#pragma unroll
    for (int u = 0; u < 4; ++u) {
      const int i = tid + u * 448;
      if (isU) vx[kh][u] = ldg8(p.Wo + ((size_t)h * E_ + kh * 128 + i / 14) * D_ + tr * 112 + (i % 14) * 8);
      else vx[kh][u] = ldg8(p.Wq + ((size_t)(tr * 112 + (i >> 4)) * H_ + h) * E_ + kh * 128 + (i & 15) * 8);
    }
#pragma unroll
    for (int u = 0; u < 8; ++u) {
      const int i = tid + u * 448;
      vy[kh][u] = ldg8(Yop + ((size_t)(tc * 224 + (i >> 4)) * H_ + h) * E_ + kh * 128 + (i & 15) * 8);
    }
  }
  {
    constexpr unsigned N16 = (unsigned)((SZ_HAND - M_ * D_ * 2) / 16);
    u32x4 f = {SENT, SENT, SENT, SENT};
    u32x4* dst = (u32x4*)(p.ws + M_ * D_ * 2);
    for (unsigned i = b * 448 + tid; i < N16; i += 256 * 448) dst[i] = f;
  }
  if (b == 255) {
    u16* memb = (u16*)(p.ws + OFF_MEMB);
    for (int i = tid; i < M_ * D_; i += 448) { int r = i / D_, d = i % D_; memb[((d >> 3) * 8 + r) * 8 + (d & 7)] = p.mem0[i]; }
    for (int i = tid; i < CNT_N; i += 448) *(unsigned*)(p.ws + OFF_CNT + (size_t)i * 64) = 0u;
  }
  for (int rr = w; rr < 8; rr += 7) {
    const int row = b * 8 + rr;
    float v[8]; float s = 0.f;
    if (lane < 56) {
      bf16x8 xv = ldg8(p.x + (size_t)row * D_ + lane * 8);
#pragma unroll
      for (int i = 0; i < 8; ++i) { v[i] = bf2f((u16)xv[i]); s += v[i]; }
    } else {
#pragma unroll
      for (int i = 0; i < 8; ++i) v[i] = 0.f;
    }
#pragma unroll
    for (int o = 32; o > 0; o >>= 1) s += __shfl_xor(s, o);
    const float mean = s * (1.f / D_);
    float q = 0.f;
    if (lane < 56) {
#pragma unroll
      for (int i = 0; i < 8; ++i) { float d = v[i] - mean; q += d * d; }
    }
#pragma unroll
    for (int o = 32; o > 0; o >>= 1) q += __shfl_xor(q, o);
    const float var = rbf(q * (1.f / D_));
    const float mb = rbf(mean);
    const float sd = rbf(sqrtf(rbf(var + 1e-5f)));
    if (lane < 56) {
      u32x4 o;
#pragma unroll
      for (int i = 0; i < 4; ++i) o[i] = pk2(rbf(v[2 * i] - mb) / sd, rbf(v[2 * i + 1] - mb) / sd);
      *reinterpret_cast<u32x4*>((u16*)(p.ws + OFF_XN) + (size_t)row * D_ + lane * 8) = o;
    }
  }
  f32x4 acc[7][2] = {};
#pragma unroll
  for (int kh = 0; kh < 2; ++kh) {
    if (kh) __syncthreads();
#pragma unroll
    for (int u = 0; u < 4; ++u) {
      const int i = tid + u * 448;
      if (isU) {
        const int e = i / 14, c = i % 14;
#pragma unroll
        for (int j = 0; j < 8; ++j) sx[(c * 8 + j) * PLD + ((((e >> 3) ^ c) & 15) << 3) + (e & 7)] = (u16)vx[kh][u][j];
      } else *reinterpret_cast<bf16x8*>(&sx[(i >> 4) * PLD + ((((i & 15) ^ (i >> 7)) & 15) << 3)]) = vx[kh][u];
    }
#pragma unroll
    for (int u = 0; u < 8; ++u) { const int i = tid + u * 448; *reinterpret_cast<bf16x8*>(&sy[(i >> 4) * PLD + (i & 15) * 8]) = vy[kh][u]; }
    __syncthreads();
#pragma unroll
    for (int k = 0; k < 4; ++k) {
      bf16x8 yb[2];
#pragma unroll
      for (int j = 0; j < 2; ++j) yb[j] = *reinterpret_cast<const bf16x8*>(&sy[((2 * w + j) * 16 + fr) * PLD + k * 32 + fq * 8]);
#pragma unroll
      for (int i = 0; i < 7; ++i) {
        bf16x8 xa = *reinterpret_cast<const bf16x8*>(&sx[(i * 16 + fr) * PLD + ((((k * 4 + fq) ^ (i * 2 + (fr >> 3))) & 15) << 3)]);
#pragma unroll
        for (int j = 0; j < 2; ++j) acc[i][j] = mfma32(yb[j], xa, acc[i][j]);
      }
    }
  }
  u16* WT = (u16*)(p.ws + OFF_WT);
  const float sc = isU ? 1.f : 0.0625f * 1.4426950408889634f;
#pragma unroll
  for (int i = 0; i < 7; ++i)
#pragma unroll
    for (int j = 0; j < 2; ++j) {
      const int row = (isU ? HD : 0) + h * D_ + tr * 112 + i * 16 + fr;
      const int col = tc * 224 + (2 * w + j) * 16 + fq * 4;
      *reinterpret_cast<bf16x4*>(WT + (size_t)row * D_ + col) = cvt4(acc[i][j] * sc);
    }
}

constexpr int XS_LD = 456;
template <int NROWS>
__device__ __forceinline__ void stage_x_t(const Params& p, u16* xs, int row0) {
  const u16* xn = (const u16*)(p.ws + OFF_XN) + (size_t)row0 * D_;
  constexpr int N = NROWS * 56, IT = (N + 511) / 512;
  const int t = otid();
  bf16x8 v[IT];
#pragma unroll
  for (int u = 0; u < IT; ++u) { const int i = t + u * 512; if (i < N) v[u] = ldg8(xn + (size_t)i * 8); }
#pragma unroll
  for (int u = 0; u < IT; ++u) { const int i = t + u * 512; if (i < N) { const int r = i / 56, c = i % 56; *reinterpret_cast<bf16x8*>(&xs[r * XS_LD + c * 8]) = v[u]; } }
}

template <bool ISG>
__device__ __forceinline__ void producer_t(const Params& p, rsrc_t rs, char* smem, int nt) {
  const int tid = otid(), lane = tid & 63, w = tid >> 6, fr = lane & 15, fq = lane >> 4;
  u16* xs = (u16*)smem;
  u16* ot = xs + 2 * 32 * XS_LD;
  const u16* WT = (const u16*)(p.ws + OFF_WT);
  const u16* xn = (const u16*)(p.ws + OFF_XN);
  bf16x8 wb[14];
#pragma unroll
  for (int k = 0; k < 14; ++k) wb[k] = ldg8(WT + (size_t)(nt * 128 + w * 16 + fr) * D_ + k * 32 + fq * 8);
  bf16x8 xv[4];
#pragma unroll
  for (int u = 0; u < 4; ++u) { const int i = tid + u * 512; if (i < 1792) xv[u] = ldg8(xn + (size_t)i * 8); }
  for (int s = 0; s < NSEG; ++s) {
    u16* xb = xs + (s & 1) * 32 * XS_LD;
#pragma unroll
    for (int u = 0; u < 4; ++u) { const int i = tid + u * 512; if (i < 1792) { const int r = i / 56, c = i % 56; *reinterpret_cast<bf16x8*>(&xb[r * XS_LD + c * 8]) = xv[u]; } }
    asm volatile("s_waitcnt vmcnt(1)" ::: "memory");
    __syncthreads();
    if (s > 5 && tid == 0) __hip_atomic_fetch_add(cnt_ptr(p, CNT_GY + s - 2), 1u, __ATOMIC_RELAXED, __HIP_MEMORY_SCOPE_AGENT);
    if (s + 1 < NSEG) {
#pragma unroll
      for (int u = 0; u < 4; ++u) { const int i = tid + u * 512; if (i < 1792) xv[u] = ldg8(xn + (size_t)(s + 1) * 32 * D_ + (size_t)i * 8); }
    }
    f32x4 acc[2] = {};
#pragma unroll
    for (int k = 0; k < 14; ++k)
#pragma unroll
      for (int i = 0; i < 2; ++i) {
        bf16x8 xa = *reinterpret_cast<const bf16x8*>(&xb[(i * 16 + fr) * XS_LD + k * 32 + fq * 8]);
        acc[i] = ISG ? mfma32(wb[k], xa, acc[i]) : mfma32(xa, wb[k], acc[i]);
      }
    if (ISG) {
#pragma unroll
      for (int i = 0; i < 2; ++i) *reinterpret_cast<u32x2*>(&ot[(i * 16 + fr) * 136 + w * 16 + fq * 4]) = cvt4u(acc[i]);
    } else {
#pragma unroll
      for (int i = 0; i < 2; ++i) *reinterpret_cast<u32x2*>(&ot[(w * 16 + fr) * 36 + i * 16 + fq * 4]) = cvt4u(acc[i]);
    }
    __syncthreads();
    if (ISG) {
      const int r = tid >> 4, cch = tid & 15;
      const u32x2 a = *reinterpret_cast<const u32x2*>(&ot[r * 136 + cch * 8]), b2 = *reinterpret_cast<const u32x2*>(&ot[r * 136 + cch * 8 + 4]);
      u32x4 v = {a[0], a[1], b2[0], b2[1]};
      st128(rs, (unsigned)OFF_GT + (unsigned)((s * 32 + r) * HD + nt * 128 + cch * 8) * 2u, v);
    } else {
      const int row = tid >> 2, cch = tid & 3;
      const u32x2 a = *reinterpret_cast<const u32x2*>(&ot[row * 36 + cch * 8]), b2 = *reinterpret_cast<const u32x2*>(&ot[row * 36 + cch * 8 + 4]);
      u32x4 v = {a[0], a[1], b2[0], b2[1]};
      st128(rs, (unsigned)OFF_YT + (unsigned)((s * HD + nt * 128 - HD + row) * 32 + cch * 8) * 2u, v);
    }
    if (s < 4) signal_add(p, CNT_GY + s, 1u);
  }
  signal_add(p, CNT_GY + NSEG - 2, 1u);
  if (tid == 0) __hip_atomic_fetch_add(cnt_ptr(p, CNT_GY + NSEG - 1), 1u, __ATOMIC_RELAXED, __HIP_MEMORY_SCOPE_AGENT);
}

__device__ __forceinline__ void tr_item(const Params& p, rsrc_t rs, char* smem, int it) {
  const int tid = otid();
  u16* sx = (u16*)smem;
  for (int t = it * 8; t < it * 8 + 8; ++t) {
    const bool second = t >= 196; const int tt = second ? t - 196 : t;
    const u16* src = second ? p.w2 : p.w1;
    const unsigned dst = (unsigned)(second ? OFF_W2T : OFF_W1T);
    const int R = second ? F_ : D_, C = second ? D_ : F_;
    const int nct = C / 64, rt = tt / nct, ct = tt % nct;
    __syncthreads();
    { const int r = tid >> 3, c = tid & 7;
      bf16x8 v = ldg8(src + (size_t)(rt * 64 + r) * C + ct * 64 + c * 8);
#pragma unroll
      for (int j = 0; j < 8; ++j) sx[(c * 8 + j) * 72 + r] = (u16)v[j]; }
    __syncthreads();
    { const int c = tid >> 3, r = tid & 7;
      st128(rs, dst + (unsigned)((ct * 64 + c) * R + rt * 64 + r * 8) * 2u, *reinterpret_cast<const u32x4*>(&sx[c * 72 + r * 8])); }
  }
  signal_add(p, CNT_TR, 1u);
}

constexpr int HS_LD = 1800;
__device__ __forceinline__ float gelu_tanh(float x) {
  const float u = 0.7978845608028654f * (x + 0.044715f * x * x * x);
  const float e = __expf(2.f * u);
  const float th = 1.f - 2.f / (e + 1.f);
  return 0.5f * x * (1.f + th);
}
__device__ __forceinline__ void mlp_item(const Params& p, rsrc_t rs, char* smem, int rb) {
  const int tid = otid(), lane = tid & 63, w = tid >> 6, fr = lane & 15, fq = lane >> 4;
  u16* xs = (u16*)smem;
  u16* hs = xs + 32 * XS_LD;
  wait_cnt(p, CNT_TR, 49u);
  acquire_all();
  stage_x_t<32>(p, xs, rb * 32);
  __syncthreads();
  const u16* w1T = (const u16*)(p.ws + OFF_W1T);
  for (int nt = w; nt < 112; nt += 16) {
    f32x4 acc[2][2] = {};
    bf16x8 wb[2][14];
#pragma unroll
    for (int u = 0; u < 2; ++u)
#pragma unroll
      for (int k = 0; k < 14; ++k) wb[u][k] = ldg8(w1T + (size_t)((nt + 8 * u) * 16 + fr) * D_ + k * 32 + fq * 8);
#pragma unroll
    for (int k = 0; k < 14; ++k)
#pragma unroll
      for (int i = 0; i < 2; ++i) {
        bf16x8 xa = *reinterpret_cast<const bf16x8*>(&xs[(i * 16 + fr) * XS_LD + k * 32 + fq * 8]);
#pragma unroll
        for (int u = 0; u < 2; ++u) acc[u][i] = mfma32(wb[u][k], xa, acc[u][i]);
      }
#pragma unroll
    for (int u = 0; u < 2; ++u) {
      const int n = (nt + 8 * u) * 16 + fq * 4;
      bf16x4 bv = *reinterpret_cast<const bf16x4*>(p.b1 + n);
#pragma unroll
      for (int i = 0; i < 2; ++i) {
        f32x4 g;
#pragma unroll
        for (int r = 0; r < 4; ++r) g[r] = gelu_tanh(rbf(rbf(acc[u][i][r]) + bf2f((u16)bv[r])));
        *reinterpret_cast<bf16x4*>(&hs[(i * 16 + fr) * HS_LD + n]) = cvt4(g);
      }
    }
  }
  __syncthreads();
  const u16* w2T = (const u16*)(p.ws + OFF_W2T);
  const int kh = w & 1, ng = w >> 1;
  f32x4 acc[2][7] = {};
  for (int k4 = 0; k4 < 7; ++k4) {
    bf16x8 wb[4][7];
#pragma unroll
    for (int u = 0; u < 4; ++u)
#pragma unroll
      for (int j = 0; j < 7; ++j) wb[u][j] = ldg8(w2T + (size_t)((ng * 7 + j) * 16 + fr) * F_ + kh * 896 + (k4 * 4 + u) * 32 + fq * 8);
#pragma unroll
    for (int u = 0; u < 4; ++u) {
      const int kk = kh * 896 + (k4 * 4 + u) * 32 + fq * 8;
#pragma unroll
      for (int i = 0; i < 2; ++i) {
        bf16x8 ha = *reinterpret_cast<const bf16x8*>(&hs[(i * 16 + fr) * HS_LD + kk]);
#pragma unroll
        for (int j = 0; j < 7; ++j) acc[i][j] = mfma32(wb[u][j], ha, acc[i][j]);
      }
    }
  }
  __syncthreads();
  float* red = (float*)smem;
  if (kh == 1) {
#pragma unroll
    for (int i = 0; i < 2; ++i)
#pragma unroll
      for (int j = 0; j < 7; ++j) *reinterpret_cast<f32x4*>(&red[(i * 16 + fr) * D_ + (ng * 7 + j) * 16 + fq * 4]) = acc[i][j];
  }
  __syncthreads();
  if (kh == 0) {
#pragma unroll
    for (int i = 0; i < 2; ++i)
#pragma unroll
      for (int j = 0; j < 7; ++j) {
        const int n = (ng * 7 + j) * 16 + fq * 4;
        f32x4 o = acc[i][j] + *reinterpret_cast<const f32x4*>(&red[(i * 16 + fr) * D_ + n]);
        bf16x4 bv = *reinterpret_cast<const bf16x4*>(p.b2 + n);
#pragma unroll
        for (int r = 0; r < 4; ++r) o[r] = rbf(o[r]) + bf2f((u16)bv[r]);
        st64(rs, (unsigned)OFF_MLP + (unsigned)((rb * 32 + i * 16 + fr) * D_ + n) * 2u, cvt4u(o));
      }
  }
  signal_add(p, CNT_MLP + rb, 1u);
}

__device__ __forceinline__ void sss_item(const Params& p, rsrc_t rs, int s) {
  const int tid = otid(), lane = tid & 63, w = tid >> 6, fr = lane & 15, fq = lane >> 4;
  wait_cnt(p, CNT_GY + s, 112u);
  acquire_all();
  const u16* xn = (const u16*)(p.ws + OFF_XN) + (size_t)s * 32 * D_;
  const u16* Gt = (const u16*)(p.ws + OFF_GT) + (size_t)s * 32 * HD;
  for (int hh = 0; hh < 2; ++hh) {
    const int h = w * 2 + hh;
    f32x4 acc[2][2] = {};
#pragma unroll
    for (int k = 0; k < 14; ++k) {
      bf16x8 xa[2], gb[2];
#pragma unroll
      for (int i = 0; i < 2; ++i) {
        xa[i] = ldg8(xn + (size_t)(i * 16 + fr) * D_ + k * 32 + fq * 8);
        gb[i] = ldg8(Gt + (size_t)(i * 16 + fr) * HD + h * D_ + k * 32 + fq * 8);
      }
#pragma unroll
      for (int i = 0; i < 2; ++i)
#pragma unroll
        for (int j = 0; j < 2; ++j) acc[i][j] = mfma32(gb[j], xa[i], acc[i][j]);
    }
#pragma unroll
    for (int i = 0; i < 2; ++i)
#pragma unroll
      for (int j = 0; j < 2; ++j)
        st128(rs, (unsigned)OFF_SSS + (unsigned)(((s * H_ + h) * 32 + i * 16 + fr) * 32 + j * 16 + fq * 4) * 4u, __builtin_bit_cast(u32x4, acc[i][j]));
  }
  signal_add(p, CNT_SSS + s, 1u);
}

constexpr int PS_LD = 648;
__device__ __forceinline__ void tail_item(const Params& p, rsrc_t rs, char* smem, int s, int cq) {
  const int tid = otid(), lane = tid & 63, w = tid >> 6, fr = lane & 15, fq = lane >> 4;
  float* ssm = (float*)smem;
  u16* ps = (u16*)(smem + 16 * 32 * 8 * 4);
  u16* xs = ps + 32 * PS_LD;
  wait_cnt(p, CNT_SSS + s, 1u);
  wait_cnt(p, CNT_MLP + s, 1u);
  acquire_all();
  stage_x_t<32>(p, xs, s * 32);
  const int hrow = tid >> 5, trow = tid & 31;
  f32x4 sv[8];
  {
    const float* sr = (const float*)(p.ws + OFF_SSS) + (((size_t)s * H_ + hrow) * 32 + trow) * 32;
#pragma unroll
    for (int k = 0; k < 8; ++k) sv[k] = *reinterpret_cast<const f32x4*>(sr + k * 4);
  }
  bf16x8 wb[20];
  const int dcol = cq * 112 + (w < 7 ? w : 0) * 16 + fr;
  {
    const u16* Yt = (const u16*)(p.ws + OFF_YT) + (size_t)s * HD * 32;
#pragma unroll
    for (int k = 0; k < 16; ++k) wb[k] = ldg8(Yt + ((size_t)k * D_ + dcol) * 32 + fq * 8);
  }
  f32x4 base[2];
  {
    const u16* mo = (const u16*)(p.ws + OFF_MLP);
#pragma unroll
    for (int i = 0; i < 2; ++i) {
      const size_t o = (size_t)(s * 32 + i * 16 + fr) * D_ + cq * 112 + (w < 7 ? w : 0) * 16 + fq * 4;
      u32x2 xv = *reinterpret_cast<const u32x2*>(p.x + o), bv = *reinterpret_cast<const u32x2*>(mo + o);
      base[i][0] = __uint_as_float(xv[0]); base[i][1] = __uint_as_float(xv[1]); base[i][2] = __uint_as_float(bv[0]); base[i][3] = __uint_as_float(bv[1]);
    }
  }
  if (tid == 0) {
    unsigned spins = 0;
    if (s < NSEG - 1) {
      while (__hip_atomic_load(cnt_ptr(p, CNT_REC + s), __ATOMIC_RELAXED, __HIP_MEMORY_SCOPE_AGENT) < 64u) { __builtin_amdgcn_s_sleep(127); if (spin_fail(p, spins, CAP_SLOW)) break; }
    } else {
      while (__hip_atomic_load(cnt_ptr(p, CNT_REC + s - 1), __ATOMIC_RELAXED, __HIP_MEMORY_SCOPE_AGENT) < 64u) { __builtin_amdgcn_s_sleep(127); if (spin_fail(p, spins, CAP_SLOW)) break; }
      spins = 0;
      while (__hip_atomic_load(cnt_ptr(p, CNT_REC + s), __ATOMIC_RELAXED, __HIP_MEMORY_SCOPE_AGENT) < 64u) { __builtin_amdgcn_s_sleep(2); if (spin_fail(p, spins, CAP_FAST)) break; }
    }
  }
  __syncthreads();
  {
    bf16x8 gb[14];
#pragma unroll
    for (int k = 0; k < 14; ++k) {
      const int d = k * 32 + fq * 8, hh2 = w * 2 + (fr >> 3), jq2 = d / 112, dd = d - jq2 * 112;
      gb[k] = ld8s(rs, (unsigned)OFF_GM + (unsigned)(s * (H_ * M_ * D_) + (((hh2 * 4 + jq2) * 7 + (dd >> 4)) * 8 + (fr & 7)) * 16 + (dd & 15)) * 2u);
    }
#pragma unroll
    for (int k = 16; k < 20; ++k) {
      const int hz = (k - 16) * 4 + fq;
      wb[k] = ld8s(rs, (unsigned)OFF_ZM + (unsigned)(s * (H_ * D_ * M_) + (((dcol >> 3) * 8 + (hz >> 1)) * 16 + (hz & 1) * 8 + (dcol & 7)) * 8) * 2u);
    }
    f32x4 acc[2] = {};
#pragma unroll
    for (int k = 0; k < 14; ++k)
#pragma unroll
      for (int i = 0; i < 2; ++i) {
        bf16x8 xa = *reinterpret_cast<const bf16x8*>(&xs[(i * 16 + fr) * XS_LD + k * 32 + fq * 8]);
        acc[i] = mfma32(gb[k], xa, acc[i]);
      }
#pragma unroll
    for (int i = 0; i < 2; ++i)
      *reinterpret_cast<f32x4*>(&ssm[((w * 2 + (fq >> 1)) * 32 + i * 16 + fr) * 8 + (fq & 1) * 4]) = acc[i];
  }
  __syncthreads();
  {
    const int h = hrow, t = trow;
    float a[32], m[8];
#pragma unroll
    for (int k = 0; k < 8; ++k) { a[4 * k] = sv[k][0]; a[4 * k + 1] = sv[k][1]; a[4 * k + 2] = sv[k][2]; a[4 * k + 3] = sv[k][3]; }
#pragma unroll
    for (int j = 0; j < 8; ++j) m[j] = ssm[(h * 32 + t) * 8 + j];
    float mx = m[0];
#pragma unroll
    for (int j = 1; j < 8; ++j) mx = fmaxf(mx, m[j]);
#pragma unroll
    for (int k = 0; k < 32; ++k) if (k <= t) mx = fmaxf(mx, a[k]);
    float sum = 0.f;
#pragma unroll
    for (int j = 0; j < 8; ++j) { m[j] = ex2(m[j] - mx); sum += m[j]; }
#pragma unroll
    for (int k = 0; k < 32; ++k) { a[k] = (k <= t) ? ex2(a[k] - mx) : 0.f; sum += a[k]; }
    const float inv = 1.f / sum;
#pragma unroll
    for (int k = 0; k < 4; ++k) {
      u32x4 v;
#pragma unroll
      for (int r = 0; r < 4; ++r) v[r] = pk2(a[8 * k + 2 * r] * inv, a[8 * k + 2 * r + 1] * inv);
      *reinterpret_cast<u32x4*>(&ps[t * PS_LD + h * 32 + k * 8]) = v;
    }
    u32x4 v;
#pragma unroll
    for (int r = 0; r < 4; ++r) v[r] = pk2(m[2 * r] * inv, m[2 * r + 1] * inv);
    *reinterpret_cast<u32x4*>(&ps[t * PS_LD + 512 + h * 8]) = v;
  }
  __syncthreads();
  if (w < 7) {
    f32x4 acc[2] = {};
#pragma unroll
    for (int k = 0; k < 20; ++k) {
#pragma unroll
      for (int i = 0; i < 2; ++i) {
        bf16x8 pa = *reinterpret_cast<const bf16x8*>(&ps[(i * 16 + fr) * PS_LD + k * 32 + fq * 8]);
        acc[i] = mfma32(wb[k], pa, acc[i]);
      }
    }
#pragma unroll
    for (int i = 0; i < 2; ++i) {
      const size_t o = (size_t)(s * 32 + i * 16 + fr) * D_ + cq * 112 + w * 16 + fq * 4;
      const unsigned x0 = __float_as_uint(base[i][0]), x1 = __float_as_uint(base[i][1]), b0 = __float_as_uint(base[i][2]), b1 = __float_as_uint(base[i][3]);
      f32x4 r;
      r[0] = rbf(lo16(x0) + rbf(acc[i][0])) + lo16(b0);
      r[1] = rbf(hi16(x0) + rbf(acc[i][1])) + hi16(b0);
      r[2] = rbf(lo16(x1) + rbf(acc[i][2])) + lo16(b1);
      r[3] = rbf(hi16(x1) + rbf(acc[i][3])) + hi16(b1);
      *reinterpret_cast<bf16x4*>(p.out + o) = cvt4(r);
    }
  }
  __syncthreads();
}

constexpr int IT_TR = 49, IT_MLP = 113, IT_SSS = 177, IT_END = 433;
__device__ __forceinline__ void worker(const Params& p, char* smem) {
  const rsrc_t rs = ws_rsrc(p);
  unsigned* sitem = (unsigned*)(smem + 152 * 1024 - 16);
  if (blockIdx.x < 64 + 112) {
    const int nt = blockIdx.x - 64;
    if (nt < 56) producer_t<true>(p, rs, smem, nt); else producer_t<false>(p, rs, smem, nt);
  }
  for (;;) {
    __syncthreads();
    if (threadIdx.x == 0) *sitem = __hip_atomic_fetch_add(cnt_ptr(p, CNT_Q), 1u, __ATOMIC_RELAXED, __HIP_MEMORY_SCOPE_AGENT);
    __syncthreads();
    const int it = (int)*sitem;
    if (it >= IT_END) break;
    if (it < IT_TR) tr_item(p, rs, smem, it);
    else if (it < IT_MLP) mlp_item(p, rs, smem, it - IT_TR);
    else if (it < IT_SSS) sss_item(p, rs, it - IT_MLP);
    else { const int q = it - IT_SSS; tail_item(p, rs, smem, q >> 2, q & 3); }
  }
}

__device__ __forceinline__ void recurrence(const Params& p, char* smem) {
  u16* ms = (u16*)smem;
  float* red1 = (float*)(smem + 7168);
  float* red2 = red1 + 7 * 64;
  u16* zs = (u16*)(red2 + 16 * 64);
  u32x4* gbuf = (u32x4*)(smem + 16384);
  u32x4* ybuf = (u32x4*)(smem + 16384 + 28672);
  const unsigned lds0 = __builtin_amdgcn_readfirstlane((unsigned)(size_t)(LAS char*)smem);
  const int tid = threadIdx.x, lane = tid & 63, w = tid >> 6, fr = lane & 15, fq = lane >> 4;
  const int c = blockIdx.x, h = c >> 2, jq = c & 3;
  const bool isval = c < 56;
  const u16* WT = (const u16*)(p.ws + OFF_WT);
  const rsrc_t rs = ws_rsrc(p);
  const unsigned g_off = (unsigned)OFF_GT + (unsigned)((jq * 8 + (fr & 7)) * HD + h * D_ + fq * 8) * 2u;
  const int zh = 2 * w + (fr >> 3), zd = (isval ? 8 * c : 0) + (fr & 7);

  bf16x8 wa[14], wz[14];
  if (w < 7) {
#pragma unroll
    for (int k = 0; k < 14; ++k) wa[k] = ldg8(WT + (size_t)(h * D_ + jq * 112 + w * 16 + fr) * D_ + k * 32 + fq * 8);
  }
#pragma unroll
  for (int k = 0; k < 14; ++k) wz[k] = ldg8(WT + (size_t)(HD + zh * D_ + zd) * D_ + k * 32 + fq * 8);
  auto prefetch = [&](int sg) {
    const char* gp = p.ws + g_off + (size_t)sg * (32 * HD * 2);
#pragma unroll
    for (int k = 0; k < 14; ++k) glds16_sc1(gp + k * 64, lds0 + 16384 + (sg & 1) * 14336 + k * 1024);
    if (isval) {
      const char* yp = p.ws + OFF_YT + (size_t)(((fr >> 3) * D_ + zd) * 32 + fq * 8) * 2 + (size_t)sg * (HD * 32 * 2);
#pragma unroll
      for (int k = 0; k < 8; ++k) glds16_sc1(yp + (size_t)k * (2 * D_ * 32 * 2), lds0 + 16384 + 28672 + (sg & 1) * 8192 + k * 1024);
    }
  };
  if (w == 7) {
    unsigned spins = 0;
    while (__hip_atomic_load(cnt_ptr(p, CNT_GY), __ATOMIC_RELAXED, __HIP_MEMORY_SCOPE_AGENT) < 112u) { __builtin_amdgcn_s_sleep(2); if (spin_fail(p, spins, CAP_FAST)) break; }
    prefetch(0);
  }
  __builtin_amdgcn_s_waitcnt(0);
  for (int s = 0; s < NSEG; ++s) {
    if (w < 7) {
      const int id = w * 64 + lane, cc = id >> 3, i = id & 7;
      const unsigned off = (unsigned)OFF_MEMB + s * (M_ * D_ * 2) + id * 16;
      u32x4 v; unsigned spins = 0;
      if (s > 0) { if (!isval) __builtin_amdgcn_s_sleep(SLN); __builtin_amdgcn_s_sleep(SLX); }
      for (;;) {
        asm volatile("" ::: "memory");
        v = ld128(rs, off);
        if (!__any(has_sent(v))) break;
        if (spin_fail(p, spins, CAP_FAST)) break;
      }
      *reinterpret_cast<u32x4*>(&ms[((cc >> 3) * 64 + (cc & 3) * 16 + ((cc >> 2) & 1) * 8 + i) * 8]) = v;
    } else {
      asm volatile("s_waitcnt vmcnt(0)" ::: "memory");
      if (s <= 1) {
        unsigned spins = 0;
        while (__hip_atomic_load(cnt_ptr(p, CNT_GY + s + 1), __ATOMIC_RELAXED, __HIP_MEMORY_SCOPE_AGENT) < 112u) { __builtin_amdgcn_s_sleep(2); if (spin_fail(p, spins, CAP_FAST)) break; }
      }
      if (s + 1 < NSEG) prefetch(s + 1);
#pragma unroll
      for (int k = 0; k < 14; ++k) wa[k] = __builtin_bit_cast(bf16x8, gbuf[((s & 1) * 14 + k) * 64 + lane]);
    }
    __syncthreads();
    bf16x8 mf[14];
#pragma unroll
    for (int k = 0; k < 7; ++k) mf[2 * k] = *reinterpret_cast<const bf16x8*>(&ms[(k * 64 + lane) * 8]);
    bf16x4 m4;
    { const int d0 = jq * 112 + (w < 7 ? w : 0) * 16 + fq * 4, cc = d0 >> 3;
      m4 = *reinterpret_cast<const bf16x4*>(&ms[((cc >> 3) * 64 + (cc & 3) * 16 + ((cc >> 2) & 1) * 8 + (fr & 7)) * 8 + (d0 & 4)]); }
    __builtin_amdgcn_sched_barrier(0);
#pragma unroll
    for (int k = 0; k < 7; ++k) {
      u32x4 t = __builtin_bit_cast(u32x4, mf[2 * k]), r;
#pragma unroll
      for (int e = 0; e < 4; ++e) r[e] = __builtin_amdgcn_mov_dpp(t[e], 0x128  , 0xF, 0xF, false);
      mf[2 * k + 1] = __builtin_bit_cast(bf16x8, r);
    }
    f32x4 acc = {}, acc1 = {};
#pragma unroll
    for (int k = 0; k < 7; ++k) { acc = mfma32(wa[2 * k], mf[2 * k], acc); acc1 = mfma32(wa[2 * k + 1], mf[2 * k + 1], acc1); }
    acc += acc1;
    u32x2 gbu = cvt4u(acc);
    bf16x4 gb = __builtin_bit_cast(bf16x4, gbu);
    if (w < 7) {
      f32x4 z4 = {};
      f32x4 sp = mfma16(gb, m4, z4);
      if (fr < 8 && fq < 2) *reinterpret_cast<f32x4*>(&red1[w * 64 + fr * 8 + fq * 4]) = sp;
      if (fr < 8) st64(rs, (unsigned)OFF_GM + (unsigned)((((s * 64 + c) * 7 + w) * 8 + fr) * 16 + fq * 4) * 2u, gbu);
    } else {
      if (fr < 8 && fq < 2) {
        u32x2 o; o[0] = canon(gbu[0]); o[1] = canon(gbu[1]);
        st64(rs, (unsigned)OFF_SB + s * SREC + c * 256 + fr * 16 + fq * 8, o);
      }
    }
    __syncthreads();
    if (w == 0 && lane < 32) {
      f32x2 t = *reinterpret_cast<const f32x2*>(&red1[lane * 2]);
#pragma unroll
      for (int k = 1; k < 7; ++k) t += *reinterpret_cast<const f32x2*>(&red1[k * 64 + lane * 2]);
      st32(rs, (unsigned)OFF_SB + s * SREC + c * 256 + 128 + lane * 4, canon(pk2(t[0], t[1])));
    }
    if (s > 0 && tid == 64) __hip_atomic_fetch_add(cnt_ptr(p, CNT_REC + s - 1), 1u, __ATOMIC_RELAXED, __HIP_MEMORY_SCOPE_AGENT);
    const bool need_gy = (s + 2) < NSEG && s >= 1;
    if (isval) {
      f32x4 az = {};
#pragma unroll
      for (int k = 0; k < 14; ++k) az = mfma32(mf[k], wz[k], az);
      u32x2 zbu = cvt4u(az);
      if (fq < 2) {
        st64(rs, (unsigned)OFF_ZM + (unsigned)((((s * 56 + c) * 8 + w) * 16 + fr) * 8 + fq * 4) * 2u, zbu);
        *reinterpret_cast<u32x2*>(&zs[(w * 16 + fr) * 8 + fq * 4]) = zbu;
      }
      const u32x4 yfu = ybuf[((s & 1) * 8 + w) * 64 + lane];
      u32x4 zf = {0u, 0u, 0u, 0u};
      zf[0] = *reinterpret_cast<const unsigned*>(&zs[(w * 16 + fr) * 8 + fq * 2]);
      const int hh = fr >> 3, i = fr & 7, hd = 2 * w + hh;
      const unsigned o1 = (unsigned)OFF_SB + s * SREC + (hd * 4 + fq) * 256 + i * 16;
      const unsigned o2 = (unsigned)OFF_SB + s * SREC + hd * 1024 + 128 + i * 16 + fq * 4;
      const unsigned o3 = (unsigned)OFF_CNT + (unsigned)(CNT_GY + (need_gy ? (s + 2) : 0)) * 64u;
      u32x4 a0; unsigned b0, b1, b2, b3, gc; unsigned spins = 0;
      __builtin_amdgcn_s_sleep(SLY);
      for (;;) {
        asm volatile("" ::: "memory");
        a0 = ld128(rs, o1);
        b0 = ld32(rs, o2); b1 = ld32(rs, o2 + 256); b2 = ld32(rs, o2 + 512); b3 = ld32(rs, o2 + 768);
        gc = ld32(rs, o3);
        if (!__any(has_sent(a0) || b0 == SENT || b1 == SENT || b2 == SENT || b3 == SENT || gc < 112u)) break;
        if (spin_fail(p, spins, CAP_FAST)) break;
      }
      float sg[8];
#pragma unroll
      for (int r = 0; r < 4; ++r) { sg[2 * r] = lo16(a0[r]); sg[2 * r + 1] = hi16(a0[r]); }
      float m0 = (lo16(b0) + lo16(b1)) + (lo16(b2) + lo16(b3));
      float m1 = (hi16(b0) + hi16(b1)) + (hi16(b2) + hi16(b3));
      float mx = fmaxf(fmaxf(fmaxf(sg[0], sg[1]), fmaxf(sg[2], sg[3])), fmaxf(fmaxf(sg[4], sg[5]), fmaxf(sg[6], sg[7])));
      mx = fmaxf(mx, fmaxf(m0, m1));
      mx = xmax32(xmax16(mx));
      m0 = ex2(m0 - mx); m1 = ex2(m1 - mx);
      float ss = m0 + m1;
#pragma unroll
      for (int r = 0; r < 8; ++r) { sg[r] = ex2(sg[r] - mx); ss += sg[r]; }
      ss = xsum32(xsum16(ss));
      const float inv = __builtin_amdgcn_rcpf(ss);
      u32x4 pa, pm = {0u, 0u, 0u, 0u};
#pragma unroll
      for (int r = 0; r < 4; ++r) pa[r] = pk2(sg[2 * r] * inv, sg[2 * r + 1] * inv);
      pm[0] = pk2(m0 * inv, m1 * inv);
      f32x4 o = {};
      o = mfma32(__builtin_bit_cast(bf16x8, yfu), __builtin_bit_cast(bf16x8, pa), o);
      o = mfma32(__builtin_bit_cast(bf16x8, zf), __builtin_bit_cast(bf16x8, pm), o);
      if ((fr >> 3) == (fq >> 1)) *reinterpret_cast<f32x4*>(&red2[(w * 2 + (fq >> 1)) * 64 + (fr & 7) * 8 + (fq & 1) * 4]) = o;
      __syncthreads();
      if (w == 0) {
        const int hb = (lane >> 5) * 8, l2 = (lane & 31) * 2;
        f32x2 t = *reinterpret_cast<const f32x2*>(&red2[hb * 64 + l2]);
#pragma unroll
        for (int k = 1; k < 8; ++k) t += *reinterpret_cast<const f32x2*>(&red2[(hb + k) * 64 + l2]);
        const float t0 = xsum32(t[0]), t1 = xsum32(t[1]);
        if (lane < 32) st32(rs, (unsigned)OFF_MEMB + (s + 1) * (M_ * D_ * 2) + c * 128 + lane * 4, canon(pk2(t0, t1)));
      }
    } else if (need_gy) {
      unsigned spins = 0;
      while (__hip_atomic_load(cnt_ptr(p, CNT_GY + s + 2), __ATOMIC_RELAXED, __HIP_MEMORY_SCOPE_AGENT) < 112u) { __builtin_amdgcn_s_sleep(2); if (spin_fail(p, spins, CAP_FAST)) break; }
    }
  }
  signal_add(p, CNT_REC + NSEG - 1, 1u);
}

constexpr int LDS_MAIN = 152 * 1024;
__global__ __launch_bounds__(512, 2) void main_kernel(Params p) {
  extern __shared__ __attribute__((aligned(16))) char smem[];
  if (blockIdx.x < 64) recurrence(p, smem);
  else worker(p, smem);
}

extern "C" void kernel_launch(void* const* d_in, const int* in_sizes, int n_in, void* d_out, int out_size, void* d_ws,
                              size_t ws_size, hipStream_t stream) {
  Params p;
  p.x = (const u16*)d_in[0]; p.mem0 = (const u16*)d_in[1]; p.Wq = (const u16*)d_in[2]; p.Wk = (const u16*)d_in[3];
  p.Wv = (const u16*)d_in[4]; p.Wo = (const u16*)d_in[5]; p.w1 = (const u16*)d_in[8]; p.b1 = (const u16*)d_in[9];
  p.w2 = (const u16*)d_in[10]; p.b2 = (const u16*)d_in[11];
  p.out = (u16*)d_out; p.ws = (char*)d_ws;
  (void)hipFuncSetAttribute((const void*)main_kernel, hipFuncAttributeMaxDynamicSharedMemorySize, LDS_MAIN);
  prep_kernel<<<256, 448, 0, stream>>>(p);
  main_kernel<<<256, 512, LDS_MAIN, stream>>>(p);
}
```
